```python
import torch

def _softmax(x, axis=-1):
    return torch.softmax(x, dim=axis)
def _gelu(x):
    return torch.nn.functional.gelu(x, approximate="tanh")
def _silu(x):
    return torch.nn.functional.silu(x)
def _sigmoid(x):
    return torch.sigmoid(x)
def _layernorm(x, axis=-1, eps=1e-5):
    m = x.mean(dim=axis, keepdim=True)
    v = x.var(dim=axis, keepdim=True, unbiased=False)
    return (x - m) / torch.sqrt(v + eps)
def _scatter_add(tgt, idx, src, axis=0):
    return tgt.clone().index_add_(axis, idx, src)
def _segment_sum(src, seg, n):
    out = torch.zeros((n,) + src.shape[1:], dtype=src.dtype)
    return out.index_add_(0, seg, src)
def _pad(x, pw):
    flat = tuple(v for pair in reversed(pw) for v in pair)
    return torch.nn.functional.pad(x, flat)

kernel_name = 'standalone__mixer_token_mlp__sampling'

def setup_inputs():

    _w = (lambda a: (lambda t: t.to(torch.bfloat16) if t.is_floating_point() else t)(torch.as_tensor(a)))
    B, S, D, F = 32, 32, 576, 4608
    return {
        "x": _w((lambda *s: torch.randn(*s, dtype=torch.bfloat16) * 0.05)(B, S, D)),
        "w1": _w((lambda *s: torch.randn(*s, dtype=torch.bfloat16) * 0.2)(S, F) * (S ** -0.5)),
        "b1": _w((lambda *s: torch.randn(*s, dtype=torch.bfloat16) * 3.2)(F) * 0.02),
        "w2": _w((lambda *s: torch.randn(*s, dtype=torch.bfloat16) * 0.05)(F, S) * (F ** -0.5)),
        "b2": _w((lambda *s: torch.randn(*s, dtype=torch.bfloat16) * 0.1)(S) * 0.02),
    }

def reference(x, w1, b1, w2, b2):
    B, S, D = x.shape
    h = _layernorm(x, axis=-1)
    u = torch.einsum("bsd,sf->bdf", h, w1) + b1
    u = _gelu(u)
    y = torch.einsum("bdf,fs->bds", u, w2) + b2
    return (lambda x, *d: x.permute(*d))(y, 0, 2, 1)
```

```cpp
#include <hip/hip_runtime.h>
#include <hip/hip_bf16.h>

static __device__ const unsigned short gelu_cells[4096] = {
0x3981,0x3982,0x3983,0x3984,0x3985,0x3986,0x3987,0x3988,0x3989,0x398a,0x398b,0x398c,0x398d,0x398e,0x398f,0x3990,
0x3991,0x3992,0x3993,0x3994,0x3995,0x3996,0x3997,0x3998,0x3999,0x399a,0x399b,0x399c,0x399d,0x399e,0x399f,0x39a0,
0x39a1,0x39a2,0x39a3,0x39a4,0x39a5,0x39a6,0x39a7,0x39a8,0x39a9,0x39aa,0x39ab,0x39ac,0x39ad,0x39ae,0x39af,0x39b0,
0x39b1,0x39b2,0x39b3,0x39b4,0x39b5,0x39b6,0x39b7,0x39b8,0x39b9,0x39ba,0x39bb,0x39bc,0x39bd,0x39be,0x39bf,0x39c0,
0x39c1,0x39c2,0x39c3,0x39c4,0x39c5,0x39c6,0x39c7,0x39c8,0x39c9,0x39ca,0x39cb,0x39cc,0x39cd,0x39ce,0x39cf,0x39d0,
0x39d1,0x39d2,0x39d3,0x39d4,0x39d5,0x39d6,0x39d7,0x39d8,0x39d9,0x39da,0x39db,0x39dc,0x39dd,0x39de,0x39df,0x39e0,
0x39e1,0x39e2,0x39e3,0x39e4,0x39e5,0x39e6,0x39e7,0x39e8,0x39e9,0x39ea,0x39eb,0x39ec,0x39ed,0x39ee,0x39ef,0x39f0,
0x39f1,0x39f2,0x39f3,0x39f4,0x39f5,0x39f6,0x39f7,0x39f8,0x39f9,0x39fa,0x39fb,0x39fc,0x39fd,0x39fe,0x39ff,0x3a00,
0x3a01,0x3a02,0x3a03,0x3a04,0x3a05,0x3a06,0x3a07,0x3a08,0x3a09,0x3a0a,0x3a0b,0x3a0c,0x3a0d,0x3a0e,0x3a0f,0x3a10,
0x3a11,0x3a12,0x3a13,0x3a14,0x3a15,0x3a16,0x3a17,0x3a18,0x3a19,0x3a1a,0x3a1b,0x3a1c,0x3a1d,0x3a1e,0x3a1f,0x3a20,
0x3a21,0x3a22,0x3a23,0x3a24,0x3a25,0x3a26,0x3a27,0x3a28,0x3a29,0x3a2a,0x3a2b,0x3a2c,0x3a2d,0x3a2e,0x3a2f,0x3a30,
0x3a31,0x3a32,0x3a33,0x3a34,0x3a35,0x3a36,0x3a37,0x3a38,0x3a39,0x3a3a,0x3a3b,0x3a3c,0x3a3d,0x3a3e,0x3a3f,0x3a40,
0x3a41,0x3a42,0x3a43,0x3a44,0x3a45,0x3a46,0x3a47,0x3a48,0x3a49,0x3a4a,0x3a4b,0x3a4c,0x3a4d,0x3a4e,0x3a4f,0x3a50,
0x3a51,0x3a52,0x3a53,0x3a54,0x3a55,0x3a56,0x3a57,0x3a58,0x3a59,0x3a5a,0x3a5b,0x3a5c,0x3a5d,0x3a5e,0x3a5f,0x3a60,
0x3a61,0x3a62,0x3a63,0x3a64,0x3a65,0x3a66,0x3a67,0x3a68,0x3a69,0x3a6a,0x3a6b,0x3a6c,0x3a6d,0x3a6e,0x3a6f,0x3a70,
0x3a71,0x3a72,0x3a73,0x3a74,0x3a75,0x3a76,0x3a77,0x3a78,0x3a79,0x3a7a,0x3a7b,0x3a7c,0x3a7d,0x3a7e,0x3a7f,0x3a80,
0x3a81,0x3a82,0x3a83,0x3a84,0x3a85,0x3a86,0x3a87,0x3a88,0x3a89,0x3a8a,0x3a8b,0x3a8c,0x3a8d,0x3a8e,0x3a8f,0x3a90,
0x3a91,0x3a92,0x3a93,0x3a94,0x3a95,0x3a96,0x3a97,0x3a98,0x3a99,0x3a9a,0x3a9b,0x3a9c,0x3a9d,0x3a9e,0x3a9f,0x3aa0,
0x3aa1,0x3aa2,0x3aa3,0x3aa4,0x3aa5,0x3aa6,0x3aa7,0x3aa8,0x3aa9,0x3aaa,0x3aab,0x3aac,0x3aad,0x3aae,0x3aaf,0x3ab0,
0x3ab1,0x3ab2,0x3ab3,0x3ab4,0x3ab5,0x3ab6,0x3ab7,0x3ab8,0x3ab9,0x3aba,0x3abb,0x3abc,0x3abd,0x3abe,0x3abf,0x3ac0,
0x3ac1,0x3ac2,0x3ac3,0x3ac4,0x3ac6,0x3ac7,0x3ac8,0x3ac9,0x3aca,0x3acb,0x3acc,0x3acd,0x3ace,0x3acf,0x3ad0,0x3ad1,
0x3ad2,0x3ad3,0x3ad4,0x3ad5,0x3ad6,0x3ad7,0x3ad8,0x3ad9,0x3ada,0x3adb,0x3adc,0x3add,0x3ade,0x3adf,0x3ae0,0x3ae1,
0x3ae2,0x3ae3,0x3ae4,0x3ae5,0x3ae6,0x3ae7,0x3ae8,0x3ae9,0x3aea,0x3aeb,0x3aec,0x3aed,0x3aee,0x3aef,0x3af0,0x3af1,
0x3af2,0x3af3,0x3af4,0x3af5,0x3af6,0x3af7,0x3af8,0x3af9,0x3afa,0x3afb,0x3afc,0x3afd,0x3afe,0x3aff,0x3b00,0x3b01,
0x3b01,0x3b02,0x3b03,0x3b04,0x3b05,0x3b06,0x3b07,0x3b08,0x3b09,0x3b0a,0x3b0b,0x3b0d,0x3b0e,0x3b0f,0x3b10,0x3b11,
0x3b12,0x3b13,0x3b14,0x3b15,0x3b16,0x3b17,0x3b18,0x3b19,0x3b1a,0x3b1b,0x3b1c,0x3b1d,0x3b1e,0x3b1f,0x3b20,0x3b21,
0x3b22,0x3b23,0x3b24,0x3b25,0x3b26,0x3b27,0x3b28,0x3b29,0x3b2a,0x3b2b,0x3b2c,0x3b2d,0x3b2e,0x3b2f,0x3b30,0x3b31,
0x3b32,0x3b33,0x3b34,0x3b35,0x3b36,0x3b37,0x3b38,0x3b39,0x3b3a,0x3b3b,0x3b3c,0x3b3d,0x3b3e,0x3b3f,0x3b40,0x3b41,
0x3b42,0x3b43,0x3b44,0x3b45,0x3b47,0x3b48,0x3b49,0x3b4a,0x3b4b,0x3b4c,0x3b4d,0x3b4e,0x3b4f,0x3b50,0x3b51,0x3b52,
0x3b53,0x3b54,0x3b55,0x3b56,0x3b57,0x3b58,0x3b59,0x3b5a,0x3b5b,0x3b5c,0x3b5d,0x3b5e,0x3b5f,0x3b60,0x3b61,0x3b62,
0x3b63,0x3b64,0x3b65,0x3b66,0x3b67,0x3b68,0x3b69,0x3b6a,0x3b6b,0x3b6c,0x3b6d,0x3b6e,0x3b6f,0x3b70,0x3b71,0x3b72,
0x3b74,0x3b75,0x3b76,0x3b77,0x3b78,0x3b79,0x3b7a,0x3b7b,0x3b7c,0x3b7d,0x3b7e,0x3b7f,0x3b80,0x3b80,0x3b81,0x3b81,
0x3b82,0x3b83,0x3b84,0x3b85,0x3b86,0x3b87,0x3b88,0x3b89,0x3b8a,0x3b8b,0x3b8c,0x3b8e,0x3b8f,0x3b90,0x3b91,0x3b92,
0x3b93,0x3b94,0x3b95,0x3b96,0x3b97,0x3b98,0x3b99,0x3b9a,0x3b9b,0x3b9c,0x3b9d,0x3b9e,0x3b9f,0x3ba0,0x3ba1,0x3ba2,
0x3ba3,0x3ba4,0x3ba5,0x3ba6,0x3ba7,0x3ba8,0x3ba9,0x3baa,0x3bab,0x3bac,0x3bae,0x3baf,0x3bb0,0x3bb1,0x3bb2,0x3bb3,
0x3bb4,0x3bb5,0x3bb6,0x3bb7,0x3bb8,0x3bb9,0x3bba,0x3bbb,0x3bbc,0x3bbd,0x3bbe,0x3bbf,0x3bc0,0x3bc1,0x3bc2,0x3bc3,
0x3bc4,0x3bc5,0x3bc6,0x3bc7,0x3bc9,0x3bca,0x3bcb,0x3bcc,0x3bcd,0x3bce,0x3bcf,0x3bd0,0x3bd1,0x3bd2,0x3bd3,0x3bd4,
0x3bd5,0x3bd6,0x3bd7,0x3bd8,0x3bd9,0x3bda,0x3bdb,0x3bdc,0x3bdd,0x3bde,0x3bdf,0x3be1,0x3be2,0x3be3,0x3be4,0x3be5,
0x3be6,0x3be7,0x3be8,0x3be9,0x3bea,0x3beb,0x3bec,0x3bed,0x3bee,0x3bef,0x3bf0,0x3bf1,0x3bf2,0x3bf3,0x3bf4,0x3bf5,
0x3bf7,0x3bf8,0x3bf9,0x3bfa,0x3bfb,0x3bfc,0x3bfd,0x3bfe,0x3bff,0x3c00,0x3c01,0x3c01,0x3c02,0x3c02,0x3c03,0x3c03,
0x3c04,0x3c05,0x3c06,0x3c07,0x3c08,0x3c09,0x3c0a,0x3c0b,0x3c0c,0x3c0d,0x3c0e,0x3c10,0x3c11,0x3c12,0x3c13,0x3c14,
0x3c15,0x3c16,0x3c17,0x3c18,0x3c19,0x3c1a,0x3c1b,0x3c1c,0x3c1d,0x3c1e,0x3c1f,0x3c21,0x3c22,0x3c23,0x3c24,0x3c25,
0x3c26,0x3c27,0x3c28,0x3c29,0x3c2a,0x3c2b,0x3c2c,0x3c2d,0x3c2e,0x3c2f,0x3c31,0x3c32,0x3c33,0x3c34,0x3c35,0x3c36,
0x3c37,0x3c38,0x3c39,0x3c3a,0x3c3b,0x3c3c,0x3c3d,0x3c3e,0x3c40,0x3c41,0x3c42,0x3c43,0x3c44,0x3c45,0x3c46,0x3c47,
0x3c48,0x3c49,0x3c4a,0x3c4b,0x3c4d,0x3c4e,0x3c4f,0x3c50,0x3c51,0x3c52,0x3c53,0x3c54,0x3c55,0x3c56,0x3c57,0x3c58,
0x3c5a,0x3c5b,0x3c5c,0x3c5d,0x3c5e,0x3c5f,0x3c60,0x3c61,0x3c62,0x3c63,0x3c64,0x3c66,0x3c67,0x3c68,0x3c69,0x3c6a,
0x3c6b,0x3c6c,0x3c6d,0x3c6e,0x3c6f,0x3c70,0x3c72,0x3c73,0x3c74,0x3c75,0x3c76,0x3c77,0x3c78,0x3c79,0x3c7a,0x3c7b,
0x3c7d,0x3c7e,0x3c7f,0x3c80,0x3c80,0x3c81,0x3c82,0x3c82,0x3c83,0x3c83,0x3c84,0x3c84,0x3c85,0x3c85,0x3c86,0x3c87,
0x3c87,0x3c88,0x3c8a,0x3c8b,0x3c8c,0x3c8d,0x3c8e,0x3c8f,0x3c90,0x3c91,0x3c92,0x3c94,0x3c95,0x3c96,0x3c97,0x3c98,
0x3c99,0x3c9a,0x3c9b,0x3c9d,0x3c9e,0x3c9f,0x3ca0,0x3ca1,0x3ca2,0x3ca3,0x3ca4,0x3ca6,0x3ca7,0x3ca8,0x3ca9,0x3caa,
0x3cab,0x3cac,0x3cad,0x3caf,0x3cb0,0x3cb1,0x3cb2,0x3cb3,0x3cb4,0x3cb5,0x3cb7,0x3cb8,0x3cb9,0x3cba,0x3cbb,0x3cbc,
0x3cbd,0x3cbf,0x3cc0,0x3cc1,0x3cc2,0x3cc3,0x3cc4,0x3cc5,0x3cc7,0x3cc8,0x3cc9,0x3cca,0x3ccb,0x3ccc,0x3cce,0x3ccf,
0x3cd0,0x3cd1,0x3cd2,0x3cd3,0x3cd5,0x3cd6,0x3cd7,0x3cd8,0x3cd9,0x3cda,0x3cdc,0x3cdd,0x3cde,0x3cdf,0x3ce0,0x3ce1,
0x3ce3,0x3ce4,0x3ce5,0x3ce6,0x3ce7,0x3ce8,0x3cea,0x3ceb,0x3cec,0x3ced,0x3cee,0x3cef,0x3cf1,0x3cf2,0x3cf3,0x3cf4,
0x3cf5,0x3cf7,0x3cf8,0x3cf9,0x3cfa,0x3cfb,0x3cfd,0x3cfe,0x3cff,0x3d00,0x3d01,0x3d01,0x3d02,0x3d02,0x3d03,0x3d04,
0x3d04,0x3d05,0x3d05,0x3d06,0x3d07,0x3d07,0x3d08,0x3d08,0x3d09,0x3d0a,0x3d0a,0x3d0b,0x3d0b,0x3d0c,0x3d0d,0x3d0d,
0x3d0e,0x3d0f,0x3d11,0x3d12,0x3d13,0x3d14,0x3d15,0x3d17,0x3d18,0x3d19,0x3d1a,0x3d1c,0x3d1d,0x3d1e,0x3d1f,0x3d21,
0x3d22,0x3d23,0x3d24,0x3d26,0x3d27,0x3d28,0x3d29,0x3d2b,0x3d2c,0x3d2d,0x3d2e,0x3d30,0x3d31,0x3d32,0x3d33,0x3d35,
0x3d36,0x3d37,0x3d38,0x3d3a,0x3d3b,0x3d3c,0x3d3d,0x3d3f,0x3d40,0x3d41,0x3d43,0x3d44,0x3d45,0x3d46,0x3d48,0x3d49,
0x3d4a,0x3d4c,0x3d4d,0x3d4e,0x3d4f,0x3d51,0x3d52,0x3d53,0x3d55,0x3d56,0x3d57,0x3d59,0x3d5a,0x3d5b,0x3d5c,0x3d5e,
0x3d5f,0x3d60,0x3d62,0x3d63,0x3d64,0x3d66,0x3d67,0x3d68,0x3d6a,0x3d6b,0x3d6c,0x3d6e,0x3d6f,0x3d70,0x3d72,0x3d73,
0x3d74,0x3d76,0x3d77,0x3d78,0x3d7a,0x3d7b,0x3d7c,0x3d7e,0x3d7f,0x3d80,0x3d81,0x3d82,0x3d82,0x3d83,0x3d84,0x3d84,
0x3d85,0x3d86,0x3d86,0x3d87,0x3d88,0x3d88,0x3d89,0x3d8a,0x3d8a,0x3d8b,0x3d8c,0x3d8d,0x3d8d,0x3d8e,0x3d8f,0x3d8f,
0x3d90,0x3d91,0x3d91,0x3d92,0x3d93,0x3d94,0x3d94,0x3d95,0x3d96,0x3d96,0x3d97,0x3d98,0x3d98,0x3d99,0x3d9a,0x3d9b,
0x3d9c,0x3d9d,0x3d9e,0x3da0,0x3da1,0x3da3,0x3da4,0x3da6,0x3da7,0x3da9,0x3daa,0x3dab,0x3dad,0x3dae,0x3db0,0x3db1,
0x3db3,0x3db4,0x3db6,0x3db7,0x3db9,0x3dba,0x3dbc,0x3dbd,0x3dbf,0x3dc0,0x3dc2,0x3dc3,0x3dc5,0x3dc6,0x3dc8,0x3dc9,
0x3dcb,0x3dcc,0x3dce,0x3dcf,0x3dd1,0x3dd2,0x3dd4,0x3dd5,0x3dd7,0x3dd8,0x3dda,0x3ddb,0x3ddd,0x3dde,0x3de0,0x3de2,
0x3de3,0x3de5,0x3de6,0x3de8,0x3de9,0x3deb,0x3ded,0x3dee,0x3df0,0x3df1,0x3df3,0x3df4,0x3df6,0x3df8,0x3df9,0x3dfb,
0x3dfc,0x3dfe,0x3e00,0x3e01,0x3e01,0x3e02,0x3e03,0x3e04,0x3e05,0x3e06,0x3e06,0x3e07,0x3e08,0x3e09,0x3e0a,0x3e0a,
0x3e0b,0x3e0c,0x3e0d,0x3e0e,0x3e0f,0x3e0f,0x3e10,0x3e11,0x3e12,0x3e13,0x3e14,0x3e14,0x3e15,0x3e16,0x3e17,0x3e18,
0x3e19,0x3e1a,0x3e1a,0x3e1b,0x3e1c,0x3e1d,0x3e1e,0x3e1f,0x3e1f,0x3e20,0x3e21,0x3e22,0x3e23,0x3e24,0x3e25,0x3e26,
0x3e26,0x3e27,0x3e28,0x3e29,0x3e2a,0x3e2b,0x3e2c,0x3e2c,0x3e2d,0x3e2e,0x3e2f,0x3e30,0x3e31,0x3e32,0x3e33,0x3e34,
0x3e35,0x3e37,0x3e38,0x3e3a,0x3e3c,0x3e3e,0x3e40,0x3e41,0x3e43,0x3e45,0x3e47,0x3e49,0x3e4b,0x3e4c,0x3e4e,0x3e50,
0x3e52,0x3e54,0x3e56,0x3e58,0x3e59,0x3e5b,0x3e5d,0x3e5f,0x3e61,0x3e63,0x3e65,0x3e67,0x3e68,0x3e6a,0x3e6c,0x3e6e,
0x3e70,0x3e72,0x3e74,0x3e76,0x3e78,0x3e7a,0x3e7c,0x3e7e,0x3e80,0x3e81,0x3e82,0x3e83,0x3e84,0x3e85,0x3e86,0x3e87,
0x3e88,0x3e89,0x3e8a,0x3e8b,0x3e8c,0x3e8d,0x3e8e,0x3e8f,0x3e90,0x3e91,0x3e92,0x3e93,0x3e94,0x3e95,0x3e96,0x3e97,
0x3e98,0x3e99,0x3e9a,0x3e9b,0x3e9c,0x3e9d,0x3e9e,0x3e9f,0x3ea0,0x3ea1,0x3ea2,0x3ea3,0x3ea4,0x3ea5,0x3ea6,0x3ea7,
0x3ea8,0x3ea9,0x3eaa,0x3eab,0x3ead,0x3eae,0x3eaf,0x3eb0,0x3eb1,0x3eb2,0x3eb3,0x3eb4,0x3eb5,0x3eb6,0x3eb7,0x3eb8,
0x3eb9,0x3eba,0x3ebb,0x3ebc,0x3ebe,0x3ebf,0x3ec0,0x3ec1,0x3ec2,0x3ec3,0x3ec4,0x3ec5,0x3ec6,0x3ec7,0x3ec8,0x3ec9,
0x3eca,0x3ecc,0x3ecd,0x3ece,0x3ecf,0x3ed0,0x3ed1,0x3ed2,0x3ed3,0x3ed4,0x3ed5,0x3ed6,0x3ed8,0x3ed9,0x3eda,0x3edb,
0x3edc,0x3edf,0x3ee1,0x3ee3,0x3ee5,0x3ee7,0x3eea,0x3eec,0x3eee,0x3ef0,0x3ef3,0x3ef5,0x3ef7,0x3ef9,0x3efb,0x3efe,
0x3f00,0x3f01,0x3f02,0x3f03,0x3f04,0x3f06,0x3f07,0x3f08,0x3f09,0x3f0a,0x3f0b,0x3f0c,0x3f0d,0x3f0f,0x3f10,0x3f11,
0x3f12,0x3f13,0x3f14,0x3f15,0x3f16,0x3f18,0x3f19,0x3f1a,0x3f1b,0x3f1c,0x3f1d,0x3f1e,0x3f1f,0x3f21,0x3f22,0x3f23,
0x3f24,0x3f25,0x3f26,0x3f27,0x3f29,0x3f2a,0x3f2b,0x3f2c,0x3f2d,0x3f2e,0x3f2f,0x3f30,0x3f32,0x3f33,0x3f34,0x3f35,
0x3f36,0x3f37,0x3f38,0x3f39,0x3f3b,0x3f3c,0x3f3d,0x3f3e,0x3f3f,0x3f40,0x3f41,0x3f42,0x3f43,0x3f45,0x3f46,0x3f47,
0x3f48,0x3f49,0x3f4a,0x3f4b,0x3f4c,0x3f4d,0x3f4f,0x3f50,0x3f51,0x3f52,0x3f53,0x3f54,0x3f55,0x3f56,0x3f57,0x3f59,
0x3f5a,0x3f5b,0x3f5c,0x3f5d,0x3f5e,0x3f5f,0x3f60,0x3f61,0x3f62,0x3f63,0x3f65,0x3f66,0x3f67,0x3f68,0x3f69,0x3f6a,
0x3f6b,0x3f6c,0x3f6d,0x3f6e,0x3f6f,0x3f71,0x3f72,0x3f73,0x3f74,0x3f75,0x3f76,0x3f77,0x3f78,0x3f79,0x3f7a,0x3f7b,
0x3f7d,0x3f7f,0x3f81,0x3f82,0x3f83,0x3f84,0x3f85,0x3f86,0x3f87,0x3f88,0x3f89,0x3f8a,0x3f8b,0x3f8c,0x3f8d,0x3f8e,
0x3f8f,0x3f90,0x3f91,0x3f92,0x3f94,0x3f95,0x3f96,0x3f97,0x3f98,0x3f99,0x3f9a,0x3f9b,0x3f9c,0x3f9d,0x3f9e,0x3f9f,
0x3fa0,0x3fa1,0x3fa2,0x3fa3,0x3fa4,0x3fa5,0x3fa6,0x3fa7,0x3fa8,0x3fa9,0x3faa,0x3fab,0x3fac,0x3fad,0x3fae,0x3faf,
0x3fb0,0x3fb1,0x3fb2,0x3fb3,0x3fb4,0x3fb5,0x3fb6,0x3fb7,0x3fb8,0x3fb9,0x3fba,0x3fbb,0x3fbc,0x3fbd,0x3fbe,0x3fbf,
0x3fc0,0x3fc1,0x3fc2,0x3fc3,0x3fc4,0x3fc5,0x3fc6,0x3fc7,0x3fc8,0x3fc9,0x3fca,0x3fcb,0x3fcc,0x3fcd,0x3fce,0x3fcf,
0x3fd0,0x3fd1,0x3fd2,0x3fd3,0x3fd4,0x3fd5,0x3fd6,0x3fd7,0x3fd8,0x3fd9,0x3fda,0x3fdb,0x3fdc,0x3fdd,0x3fde,0x3fdf,
0x3fe0,0x3fe1,0x3fe2,0x3fe3,0x3fe4,0x3fe5,0x3fe6,0x3fe7,0x3fe8,0x3fe9,0x3fea,0x3feb,0x3fec,0x3fed,0x3fee,0x3fef,
0x3ff0,0x3ff1,0x3ff2,0x3ff3,0x3ff4,0x3ff5,0x3ff6,0x3ff7,0x3ff8,0x3ff9,0x3ffa,0x3ffb,0x3ffc,0x3ffd,0x3ffe,0x3fff,
0x4000,0x4001,0x4002,0x4003,0x4004,0x4005,0x4006,0x4007,0x4008,0x4009,0x400a,0x400b,0x400c,0x400d,0x400e,0x400f,
0x4010,0x4011,0x4012,0x4013,0x4014,0x4015,0x4016,0x4018,0x4018,0x401a,0x401a,0x401c,0x401c,0x401e,0x401e,0x4020,
0x4020,0x4022,0x4022,0x4024,0x4024,0x4026,0x4026,0x4028,0x4028,0x402a,0x402a,0x402c,0x402c,0x402e,0x402e,0x4030,
0x4030,0x4032,0x4032,0x4034,0x4034,0x4036,0x4036,0x4038,0x4038,0x403a,0x403a,0x403c,0x403c,0x403e,0x403e,0x4040,
0x4040,0x4042,0x4042,0x4044,0x4044,0x4046,0x4046,0x4048,0x4048,0x404a,0x404a,0x404c,0x404c,0x404e,0x404e,0x4050,
0x4050,0x4052,0x4052,0x4054,0x4054,0x4056,0x4056,0x4058,0x4058,0x405a,0x405a,0x405c,0x405c,0x405e,0x405e,0x4060,
0x4060,0x4062,0x4062,0x4064,0x4064,0x4066,0x4066,0x4068,0x4068,0x406a,0x406a,0x406c,0x406c,0x406e,0x406e,0x4070,
0x4070,0x4072,0x4072,0x4074,0x4074,0x4076,0x4076,0x4078,0x4078,0x407a,0x407a,0x407c,0x407c,0x407e,0x407e,0x4080,
0x4080,0x4082,0x4082,0x4084,0x4084,0x4086,0x4086,0x4088,0x4088,0x408a,0x408a,0x408c,0x408c,0x408e,0x408e,0x4090,
0x4090,0x4092,0x4092,0x4094,0x4094,0x4096,0x4096,0x4098,0x4098,0x409a,0x409a,0x409c,0x409c,0x409e,0x409e,0x40a0,
0x40a0,0x40a2,0x40a2,0x40a4,0x40a4,0x40a6,0x40a6,0x40a8,0x40a8,0x40aa,0x40aa,0x40ac,0x40ac,0x40ae,0x40ae,0x40b0,
0x40b0,0x40b2,0x40b2,0x40b4,0x40b4,0x40b6,0x40b6,0x40b8,0x40b8,0x40ba,0x40ba,0x40bc,0x40bc,0x40be,0x40be,0x40c0,
0x40c0,0x40c2,0x40c2,0x40c4,0x40c4,0x40c6,0x40c6,0x40c8,0x40c8,0x40ca,0x40ca,0x40cc,0x40cc,0x40ce,0x40ce,0x40d0,
0x40d0,0x40d2,0x40d2,0x40d4,0x40d4,0x40d6,0x40d6,0x40d8,0x40d8,0x40da,0x40da,0x40dc,0x40dc,0x40de,0x40de,0x40e0,
0x40e0,0x40e2,0x40e2,0x40e4,0x40e4,0x40e6,0x40e6,0x40e8,0x40e8,0x40ea,0x40ea,0x40ec,0x40ec,0x40ee,0x40ee,0x40f0,
0x40f0,0x40f2,0x40f2,0x40f4,0x40f4,0x40f6,0x40f6,0x40f8,0x40f8,0x40fa,0x40fa,0x40fc,0x40fc,0x40fe,0x40fe,0x4100,
0x4100,0x4102,0x4102,0x4104,0x4104,0x4106,0x4106,0x4108,0x4108,0x410a,0x410a,0x410c,0x410c,0x410e,0x410e,0x4110,
0x4110,0x4112,0x4112,0x4114,0x4114,0x4116,0x4116,0x4118,0x4118,0x411a,0x411a,0x411c,0x411c,0x411e,0x411e,0x4120,
0x4120,0x4122,0x4122,0x4124,0x4124,0x4126,0x4126,0x4128,0x4128,0x412a,0x412a,0x412c,0x412c,0x412e,0x412e,0x4130,
0x4130,0x4132,0x4132,0x4134,0x4134,0x4136,0x4136,0x4138,0x4138,0x413a,0x413a,0x413c,0x413c,0x413e,0x413e,0x4140,
0x4140,0x4142,0x4142,0x4144,0x4144,0x4146,0x4146,0x4148,0x4148,0x414a,0x414a,0x414c,0x414c,0x414e,0x414e,0x4150,
0x4150,0x4152,0x4152,0x4154,0x4154,0x4156,0x4156,0x4158,0x4158,0x415a,0x415a,0x415c,0x415c,0x415e,0x415e,0x4160,
0x4160,0x4162,0x4162,0x4164,0x4164,0x4166,0x4166,0x4168,0x4168,0x416a,0x416a,0x416c,0x416c,0x416e,0x416e,0x4170,
0x4170,0x4172,0x4172,0x4174,0x4174,0x4176,0x4176,0x4178,0x4178,0x417a,0x417a,0x417c,0x417c,0x417e,0x417e,0x4180,
0x4180,0x4182,0x4182,0x4184,0x4184,0x4186,0x4186,0x4188,0x4188,0x418a,0x418a,0x418c,0x418c,0x418e,0x418e,0x4190,
0x4190,0x4192,0x4192,0x4194,0x4194,0x4196,0x4196,0x4198,0x4198,0x419a,0x419a,0x419c,0x419c,0x419e,0x419e,0x41a0,
0x41a0,0x41a2,0x41a2,0x41a4,0x41a4,0x41a6,0x41a6,0x41a8,0x41a8,0x41aa,0x41aa,0x41ac,0x41ac,0x41ae,0x41ae,0x41b0,
0x41b0,0x41b2,0x41b2,0x41b4,0x41b4,0x41b6,0x41b6,0x41b8,0x41b8,0x41ba,0x41ba,0x41bc,0x41bc,0x41be,0x41be,0x41c0,
0x41c0,0x41c2,0x41c2,0x41c4,0x41c4,0x41c6,0x41c6,0x41c8,0x41c8,0x41ca,0x41ca,0x41cc,0x41cc,0x41ce,0x41ce,0x41d0,
0x41d0,0x41d2,0x41d2,0x41d4,0x41d4,0x41d6,0x41d6,0x41d8,0x41d8,0x41da,0x41da,0x41dc,0x41dc,0x41de,0x41de,0x41e0,
0x41e0,0x41e2,0x41e2,0x41e4,0x41e4,0x41e6,0x41e6,0x41e8,0x41e8,0x41ea,0x41ea,0x41ec,0x41ec,0x41ee,0x41ee,0x41f0,
0x41f0,0x41f2,0x41f2,0x41f4,0x41f4,0x41f6,0x41f6,0x41f8,0x41f8,0x41fa,0x41fa,0x41fc,0x41fc,0x41fe,0x41fe,0x4200,
0xb980,0xb981,0xb982,0xb983,0xb984,0xb985,0xb986,0xb987,0xb988,0xb989,0xb98a,0xb98b,0xb98c,0xb98d,0xb98e,0xb98f,
0xb990,0xb991,0xb992,0xb993,0xb994,0xb995,0xb996,0xb997,0xb998,0xb999,0xb99a,0xb99b,0xb99c,0xb99d,0xb99e,0xb99f,
0xb9a0,0xb9a1,0xb9a2,0xb9a3,0xb9a4,0xb9a5,0xb9a6,0xb9a7,0xb9a8,0xb9a9,0xb9aa,0xb9ab,0xb9ac,0xb9ad,0xb9ae,0xb9af,
0xb9b0,0xb9b1,0xb9b2,0xb9b3,0xb9b4,0xb9b5,0xb9b6,0xb9b7,0xb9b8,0xb9b9,0xb9ba,0xb9bb,0xb9bc,0xb9bd,0xb9be,0xb9bf,
0xb9c0,0xb9c1,0xb9c2,0xb9c3,0xb9c4,0xb9c5,0xb9c6,0xb9c7,0xb9c8,0xb9c9,0xb9ca,0xb9cb,0xb9cc,0xb9cd,0xb9ce,0xb9cf,
0xb9d0,0xb9d1,0xb9d2,0xb9d3,0xb9d4,0xb9d5,0xb9d6,0xb9d7,0xb9d8,0xb9d9,0xb9da,0xb9db,0xb9dc,0xb9dd,0xb9de,0xb9df,
0xb9e0,0xb9e1,0xb9e2,0xb9e3,0xb9e4,0xb9e5,0xb9e6,0xb9e7,0xb9e8,0xb9e9,0xb9ea,0xb9eb,0xb9ec,0xb9ed,0xb9ee,0xb9ef,
0xb9f0,0xb9f1,0xb9f2,0xb9f3,0xb9f4,0xb9f5,0xb9f6,0xb9f7,0xb9f8,0xb9f9,0xb9fa,0xb9fb,0xb9fc,0xb9fd,0xb9fe,0xb9ff,
0xba00,0xba01,0xba02,0xba03,0xba04,0xba05,0xba06,0xba07,0xba08,0xba09,0xba0a,0xba0b,0xba0c,0xba0d,0xba0e,0xba0f,
0xba10,0xba11,0xba12,0xba13,0xba14,0xba15,0xba16,0xba17,0xba18,0xba19,0xba1a,0xba1b,0xba1c,0xba1d,0xba1e,0xba1f,
0xba20,0xba21,0xba22,0xba23,0xba24,0xba25,0xba26,0xba27,0xba28,0xba29,0xba2a,0xba2b,0xba2c,0xba2d,0xba2e,0xba2f,
0xba30,0xba31,0xba32,0xba33,0xba34,0xba35,0xba36,0xba37,0xba38,0xba39,0xba3a,0xba3b,0xba3c,0xba3d,0xba3e,0xba3f,
0xba40,0xba41,0xba42,0xba43,0xba44,0xba45,0xba46,0xba47,0xba48,0xba49,0xba4a,0xba4b,0xba4c,0xba4d,0xba4e,0xba4f,
0xba50,0xba51,0xba52,0xba53,0xba54,0xba55,0xba56,0xba57,0xba58,0xba59,0xba5a,0xba5b,0xba5c,0xba5d,0xba5e,0xba5f,
0xba60,0xba61,0xba62,0xba63,0xba64,0xba65,0xba66,0xba67,0xba68,0xba69,0xba6a,0xba6b,0xba6c,0xba6d,0xba6e,0xba6f,
0xba70,0xba71,0xba72,0xba73,0xba74,0xba75,0xba76,0xba77,0xba78,0xba79,0xba7a,0xba7b,0xba7c,0xba7d,0xba7e,0xba7f,
0xba80,0xba81,0xba82,0xba83,0xba84,0xba85,0xba86,0xba87,0xba88,0xba89,0xba8a,0xba8b,0xba8c,0xba8d,0xba8e,0xba8f,
0xba90,0xba91,0xba92,0xba93,0xba94,0xba95,0xba96,0xba97,0xba98,0xba99,0xba9a,0xba9b,0xba9c,0xba9d,0xba9e,0xba9f,
0xbaa0,0xbaa1,0xbaa2,0xbaa3,0xbaa4,0xbaa5,0xbaa6,0xbaa7,0xbaa8,0xbaa9,0xbaaa,0xbaab,0xbaac,0xbaad,0xbaae,0xbaaf,
0xbab0,0xbab1,0xbab2,0xbab3,0xbab4,0xbab5,0xbab6,0xbab7,0xbab8,0xbab9,0xbaba,0xbabb,0xbabc,0xbabd,0xbabe,0xbabf,
0xbac0,0xbac1,0xbac2,0xbac3,0xbac3,0xbac4,0xbac5,0xbac6,0xbac7,0xbac8,0xbac9,0xbaca,0xbacb,0xbacc,0xbacd,0xbace,
0xbacf,0xbad0,0xbad1,0xbad2,0xbad3,0xbad4,0xbad5,0xbad6,0xbad7,0xbad8,0xbad9,0xbada,0xbadb,0xbadc,0xbadd,0xbade,
0xbadf,0xbae0,0xbae1,0xbae2,0xbae3,0xbae4,0xbae5,0xbae6,0xbae7,0xbae8,0xbae9,0xbaea,0xbaeb,0xbaec,0xbaed,0xbaee,
0xbaef,0xbaf0,0xbaf1,0xbaf2,0xbaf3,0xbaf4,0xbaf5,0xbaf6,0xbaf7,0xbaf8,0xbaf9,0xbafa,0xbafb,0xbafc,0xbafd,0xbafe,
0xbaff,0xbb01,0xbb02,0xbb03,0xbb04,0xbb05,0xbb06,0xbb07,0xbb08,0xbb09,0xbb0a,0xbb0a,0xbb0b,0xbb0c,0xbb0d,0xbb0e,
0xbb0f,0xbb10,0xbb11,0xbb12,0xbb13,0xbb14,0xbb15,0xbb16,0xbb17,0xbb18,0xbb19,0xbb1a,0xbb1b,0xbb1c,0xbb1d,0xbb1e,
0xbb1f,0xbb20,0xbb21,0xbb22,0xbb23,0xbb24,0xbb25,0xbb26,0xbb27,0xbb28,0xbb29,0xbb2a,0xbb2b,0xbb2c,0xbb2d,0xbb2e,
0xbb2f,0xbb30,0xbb31,0xbb32,0xbb33,0xbb34,0xbb35,0xbb36,0xbb37,0xbb38,0xbb39,0xbb3a,0xbb3b,0xbb3c,0xbb3d,0xbb3e,
0xbb3f,0xbb40,0xbb41,0xbb42,0xbb42,0xbb43,0xbb44,0xbb45,0xbb46,0xbb47,0xbb48,0xbb49,0xbb4a,0xbb4b,0xbb4c,0xbb4d,
0xbb4e,0xbb4f,0xbb50,0xbb51,0xbb52,0xbb53,0xbb54,0xbb55,0xbb56,0xbb57,0xbb58,0xbb59,0xbb5a,0xbb5b,0xbb5c,0xbb5d,
0xbb5e,0xbb5f,0xbb60,0xbb61,0xbb62,0xbb63,0xbb64,0xbb65,0xbb66,0xbb67,0xbb68,0xbb69,0xbb6a,0xbb6b,0xbb6c,0xbb6d,
0xbb6d,0xbb6e,0xbb6f,0xbb70,0xbb71,0xbb72,0xbb73,0xbb74,0xbb75,0xbb76,0xbb77,0xbb78,0xbb79,0xbb7a,0xbb7b,0xbb7c,
0xbb7e,0xbb80,0xbb81,0xbb82,0xbb83,0xbb84,0xbb85,0xbb86,0xbb87,0xbb88,0xbb89,0xbb89,0xbb8a,0xbb8b,0xbb8c,0xbb8d,
0xbb8e,0xbb8f,0xbb90,0xbb91,0xbb92,0xbb93,0xbb94,0xbb95,0xbb96,0xbb97,0xbb98,0xbb99,0xbb9a,0xbb9b,0xbb9c,0xbb9d,
0xbb9e,0xbb9f,0xbba0,0xbba1,0xbba2,0xbba3,0xbba4,0xbba5,0xbba6,0xbba7,0xbba7,0xbba8,0xbba9,0xbbaa,0xbbab,0xbbac,
0xbbad,0xbbae,0xbbaf,0xbbb0,0xbbb1,0xbbb2,0xbbb3,0xbbb4,0xbbb5,0xbbb6,0xbbb7,0xbbb8,0xbbb9,0xbbba,0xbbbb,0xbbbc,
0xbbbd,0xbbbe,0xbbbf,0xbbc0,0xbbc0,0xbbc1,0xbbc2,0xbbc3,0xbbc4,0xbbc5,0xbbc6,0xbbc7,0xbbc8,0xbbc9,0xbbca,0xbbcb,
0xbbcc,0xbbcd,0xbbce,0xbbcf,0xbbd0,0xbbd1,0xbbd2,0xbbd3,0xbbd4,0xbbd5,0xbbd6,0xbbd6,0xbbd7,0xbbd8,0xbbd9,0xbbda,
0xbbdb,0xbbdc,0xbbdd,0xbbde,0xbbdf,0xbbe0,0xbbe1,0xbbe2,0xbbe3,0xbbe4,0xbbe5,0xbbe6,0xbbe7,0xbbe8,0xbbe9,0xbbea,
0xbbea,0xbbeb,0xbbec,0xbbed,0xbbee,0xbbef,0xbbf0,0xbbf1,0xbbf2,0xbbf3,0xbbf4,0xbbf5,0xbbf6,0xbbf7,0xbbf8,0xbbf9,
0xbbfa,0xbbfc,0xbbfe,0xbc00,0xbc01,0xbc02,0xbc03,0xbc04,0xbc05,0xbc06,0xbc07,0xbc07,0xbc08,0xbc09,0xbc0a,0xbc0b,
0xbc0c,0xbc0d,0xbc0e,0xbc0f,0xbc10,0xbc11,0xbc12,0xbc13,0xbc14,0xbc15,0xbc16,0xbc16,0xbc17,0xbc18,0xbc19,0xbc1a,
0xbc1b,0xbc1c,0xbc1d,0xbc1e,0xbc1f,0xbc20,0xbc21,0xbc22,0xbc23,0xbc24,0xbc24,0xbc25,0xbc26,0xbc27,0xbc28,0xbc29,
0xbc2a,0xbc2b,0xbc2c,0xbc2d,0xbc2e,0xbc2f,0xbc30,0xbc31,0xbc31,0xbc32,0xbc33,0xbc34,0xbc35,0xbc36,0xbc37,0xbc38,
0xbc39,0xbc3a,0xbc3b,0xbc3c,0xbc3c,0xbc3d,0xbc3e,0xbc3f,0xbc40,0xbc41,0xbc42,0xbc43,0xbc44,0xbc45,0xbc46,0xbc47,
0xbc47,0xbc48,0xbc49,0xbc4a,0xbc4b,0xbc4c,0xbc4d,0xbc4e,0xbc4f,0xbc50,0xbc51,0xbc51,0xbc52,0xbc53,0xbc54,0xbc55,
0xbc56,0xbc57,0xbc58,0xbc59,0xbc5a,0xbc5b,0xbc5b,0xbc5c,0xbc5d,0xbc5e,0xbc5f,0xbc60,0xbc61,0xbc62,0xbc63,0xbc64,
0xbc64,0xbc65,0xbc66,0xbc67,0xbc68,0xbc69,0xbc6a,0xbc6b,0xbc6c,0xbc6d,0xbc6d,0xbc6e,0xbc6f,0xbc70,0xbc71,0xbc72,
0xbc73,0xbc75,0xbc77,0xbc79,0xbc7a,0xbc7c,0xbc7e,0xbc80,0xbc81,0xbc82,0xbc83,0xbc83,0xbc84,0xbc85,0xbc86,0xbc87,
0xbc88,0xbc89,0xbc8a,0xbc8a,0xbc8b,0xbc8c,0xbc8d,0xbc8e,0xbc8f,0xbc90,0xbc91,0xbc91,0xbc92,0xbc93,0xbc94,0xbc95,
0xbc96,0xbc97,0xbc98,0xbc98,0xbc99,0xbc9a,0xbc9b,0xbc9c,0xbc9d,0xbc9e,0xbc9e,0xbc9f,0xbca0,0xbca1,0xbca2,0xbca3,
0xbca4,0xbca4,0xbca5,0xbca6,0xbca7,0xbca8,0xbca9,0xbcaa,0xbcaa,0xbcab,0xbcac,0xbcad,0xbcae,0xbcaf,0xbcaf,0xbcb0,
0xbcb1,0xbcb2,0xbcb3,0xbcb4,0xbcb4,0xbcb5,0xbcb6,0xbcb7,0xbcb8,0xbcb9,0xbcb9,0xbcba,0xbcbb,0xbcbc,0xbcbd,0xbcbe,
0xbcbe,0xbcbf,0xbcc0,0xbcc1,0xbcc2,0xbcc3,0xbcc3,0xbcc4,0xbcc5,0xbcc6,0xbcc7,0xbcc8,0xbcc8,0xbcc9,0xbcca,0xbccb,
0xbccc,0xbccc,0xbccd,0xbcce,0xbccf,0xbcd0,0xbcd0,0xbcd1,0xbcd2,0xbcd3,0xbcd4,0xbcd5,0xbcd5,0xbcd6,0xbcd7,0xbcd8,
0xbcd9,0xbcd9,0xbcda,0xbcdb,0xbcdc,0xbcdd,0xbcdd,0xbcde,0xbcdf,0xbce0,0xbce0,0xbce1,0xbce2,0xbce3,0xbce4,0xbce4,
0xbce6,0xbce7,0xbce9,0xbcea,0xbcec,0xbced,0xbcef,0xbcf1,0xbcf2,0xbcf4,0xbcf5,0xbcf7,0xbcf8,0xbcfa,0xbcfb,0xbcfd,
0xbcfe,0xbd00,0xbd01,0xbd01,0xbd02,0xbd03,0xbd04,0xbd04,0xbd05,0xbd06,0xbd07,0xbd07,0xbd08,0xbd09,0xbd0a,0xbd0a,
0xbd0b,0xbd0c,0xbd0d,0xbd0d,0xbd0e,0xbd0f,0xbd10,0xbd10,0xbd11,0xbd12,0xbd12,0xbd13,0xbd14,0xbd15,0xbd15,0xbd16,
0xbd17,0xbd17,0xbd18,0xbd19,0xbd1a,0xbd1a,0xbd1b,0xbd1c,0xbd1c,0xbd1d,0xbd1e,0xbd1e,0xbd1f,0xbd20,0xbd21,0xbd21,
0xbd22,0xbd23,0xbd23,0xbd24,0xbd25,0xbd25,0xbd26,0xbd27,0xbd27,0xbd28,0xbd29,0xbd29,0xbd2a,0xbd2b,0xbd2b,0xbd2c,
0xbd2d,0xbd2d,0xbd2e,0xbd2f,0xbd2f,0xbd30,0xbd31,0xbd31,0xbd32,0xbd33,0xbd33,0xbd34,0xbd34,0xbd35,0xbd36,0xbd36,
0xbd37,0xbd38,0xbd38,0xbd39,0xbd3a,0xbd3a,0xbd3b,0xbd3b,0xbd3c,0xbd3d,0xbd3d,0xbd3e,0xbd3e,0xbd3f,0xbd40,0xbd40,
0xbd41,0xbd42,0xbd42,0xbd43,0xbd43,0xbd44,0xbd45,0xbd45,0xbd46,0xbd46,0xbd47,0xbd48,0xbd48,0xbd49,0xbd49,0xbd4a,
0xbd4b,0xbd4c,0xbd4d,0xbd4e,0xbd4f,0xbd50,0xbd52,0xbd53,0xbd54,0xbd55,0xbd56,0xbd57,0xbd58,0xbd59,0xbd5a,0xbd5c,
0xbd5d,0xbd5e,0xbd5f,0xbd60,0xbd61,0xbd62,0xbd63,0xbd64,0xbd65,0xbd66,0xbd67,0xbd68,0xbd69,0xbd6a,0xbd6b,0xbd6c,
0xbd6d,0xbd6e,0xbd6f,0xbd70,0xbd71,0xbd72,0xbd73,0xbd74,0xbd75,0xbd75,0xbd76,0xbd77,0xbd78,0xbd79,0xbd7a,0xbd7b,
0xbd7c,0xbd7d,0xbd7d,0xbd7e,0xbd7f,0xbd80,0xbd80,0xbd81,0xbd81,0xbd82,0xbd82,0xbd83,0xbd83,0xbd83,0xbd84,0xbd84,
0xbd85,0xbd85,0xbd85,0xbd86,0xbd86,0xbd86,0xbd87,0xbd87,0xbd88,0xbd88,0xbd88,0xbd89,0xbd89,0xbd89,0xbd8a,0xbd8a,
0xbd8a,0xbd8b,0xbd8b,0xbd8b,0xbd8c,0xbd8c,0xbd8c,0xbd8d,0xbd8d,0xbd8d,0xbd8e,0xbd8e,0xbd8e,0xbd8f,0xbd8f,0xbd8f,
0xbd90,0xbd90,0xbd90,0xbd91,0xbd91,0xbd91,0xbd91,0xbd92,0xbd92,0xbd92,0xbd93,0xbd93,0xbd93,0xbd93,0xbd94,0xbd94,
0xbd94,0xbd94,0xbd95,0xbd95,0xbd95,0xbd96,0xbd96,0xbd96,0xbd96,0xbd97,0xbd97,0xbd97,0xbd97,0xbd97,0xbd98,0xbd98,
0xbd98,0xbd99,0xbd99,0xbd9a,0xbd9a,0xbd9a,0xbd9b,0xbd9b,0xbd9b,0xbd9c,0xbd9c,0xbd9d,0xbd9d,0xbd9d,0xbd9e,0xbd9e,
0xbd9e,0xbd9e,0xbd9f,0xbd9f,0xbd9f,0xbd9f,0xbda0,0xbda0,0xbda0,0xbda0,0xbda1,0xbda1,0xbda1,0xbda1,0xbda1,0xbda2,
0xbda2,0xbda2,0xbda2,0xbda2,0xbda2,0xbda2,0xbda2,0xbda3,0xbda3,0xbda3,0xbda3,0xbda3,0xbda3,0xbda3,0xbda3,0xbda3,
0xbda3,0xbda3,0xbda3,0xbda3,0xbda3,0xbda3,0xbda3,0xbda3,0xbda3,0xbda3,0xbda3,0xbda3,0xbda3,0xbda3,0xbda3,0xbda3,
0xbda3,0xbda3,0xbda3,0xbda2,0xbda2,0xbda2,0xbda2,0xbda2,0xbda2,0xbda2,0xbda2,0xbda1,0xbda1,0xbda1,0xbda1,0xbda1,
0xbda1,0xbda0,0xbda0,0xbda0,0xbda0,0xbda0,0xbd9f,0xbd9f,0xbd9f,0xbd9f,0xbd9f,0xbd9e,0xbd9e,0xbd9e,0xbd9e,0xbd9d,
0xbd9d,0xbd9d,0xbd9d,0xbd9c,0xbd9c,0xbd9c,0xbd9b,0xbd9b,0xbd9b,0xbd9a,0xbd9a,0xbd9a,0xbd9a,0xbd99,0xbd99,0xbd99,
0xbd98,0xbd98,0xbd98,0xbd97,0xbd97,0xbd97,0xbd96,0xbd96,0xbd95,0xbd95,0xbd95,0xbd94,0xbd94,0xbd94,0xbd93,0xbd93,
0xbd92,0xbd91,0xbd91,0xbd90,0xbd8f,0xbd8e,0xbd8d,0xbd8c,0xbd8c,0xbd8b,0xbd8a,0xbd89,0xbd88,0xbd87,0xbd86,0xbd85,
0xbd84,0xbd83,0xbd82,0xbd81,0xbd80,0xbd7f,0xbd7d,0xbd7b,0xbd79,0xbd77,0xbd75,0xbd73,0xbd71,0xbd6f,0xbd6d,0xbd6b,
0xbd69,0xbd67,0xbd65,0xbd63,0xbd61,0xbd5f,0xbd5d,0xbd5a,0xbd58,0xbd56,0xbd54,0xbd52,0xbd50,0xbd4e,0xbd4c,0xbd4a,
0xbd48,0xbd46,0xbd44,0xbd42,0xbd40,0xbd3e,0xbd3c,0xbd3a,0xbd38,0xbd36,0xbd34,0xbd32,0xbd30,0xbd2e,0xbd2c,0xbd2a,
0xbd28,0xbd26,0xbd24,0xbd22,0xbd20,0xbd1e,0xbd1c,0xbd1a,0xbd18,0xbd16,0xbd14,0xbd13,0xbd11,0xbd0f,0xbd0d,0xbd0b,
0xbd09,0xbd08,0xbd06,0xbd04,0xbd02,0xbd01,0xbcfe,0xbcfa,0xbcf7,0xbcf3,0xbcf0,0xbced,0xbce9,0xbce6,0xbce3,0xbce0,
0xbcdc,0xbcd9,0xbcd6,0xbcd3,0xbcd0,0xbccd,0xbcca,0xbcc7,0xbcc4,0xbcc1,0xbcbe,0xbcbb,0xbcb8,0xbcb5,0xbcb2,0xbcb0,
0xbcad,0xbcaa,0xbca7,0xbca5,0xbca2,0xbc9f,0xbc9d,0xbc9a,0xbc98,0xbc95,0xbc93,0xbc90,0xbc8e,0xbc8c,0xbc89,0xbc87,
0xbc84,0xbc7e,0xbc75,0xbc6d,0xbc65,0xbc5c,0xbc55,0xbc4d,0xbc45,0xbc3e,0xbc37,0xbc30,0xbc2a,0xbc23,0xbc1d,0xbc17,
0xbc11,0xbc0b,0xbc06,0xbc00,0xbbf6,0xbbec,0xbbe3,0xbbd9,0xbbd0,0xbbc7,0xbbbf,0xbbb7,0xbbaf,0xbba7,0xbba0,0xbb99,
0xbb92,0xbb8b,0xbb85,0xbb7e,0xbb73,0xbb67,0xbb5d,0xbb52,0xbb48,0xbb3f,0xbb36,0xbb2d,0xbb24,0xbb1c,0xbb14,0xbb0d,
0xbb06,0xbafe,0xbaf1,0xbae5,0xbad9,0xbace,0xbac3,0xbab9,0xbaaf,0xbaa6,0xba9d,0xba94,0xba8c,0xba84,0xba7a,0xba6c,
0xba5f,0xba52,0xba46,0xba3b,0xba30,0xba26,0xba1c,0xba13,0xba0a,0xba02,0xb9f4,0xb9e5,0xb9d7,0xb9ca,0xb9bd,0xb9b2,
0xb9a7,0xb99c,0xb992,0xb989,0xb980,0xb970,0xb960,0xb951,0xb943,0xb936,0xb92a,0xb91f,0xb914,0xb90a,0xb901,0xb8ef,
0xb8df,0xb8cf,0xb8c1,0xb8b3,0xb8a6,0xb89a,0xb88f,0xb885,0xb877,0xb864,0xb854,0xb844,0xb835,0xb828,0xb81b,0xb80f,
0xb804,0xb7f4,0xb7e1,0xb7d0,0xb7bf,0xb7b0,0xb7a2,0xb795,0xb789,0xb77d,0xb768,0xb755,0xb744,0xb734,0xb725,0xb717,
0xb704,0xb6de,0xb6b9,0xb69a,0xb681,0xb655,0xb631,0xb612,0xb5f1,0xb5c6,0xb5a3,0xb586,0xb55a,0xb532,0xb511,0xb4eb,
0xb4bf,0xb49a,0xb478,0xb448,0xb420,0xb400,0xb3cd,0xb3a3,0xb382,0xb34e,0xb323,0xb300,0xb2ca,0xb29e,0xb278,0xb242,
0xb217,0xb1ea,0xb1b5,0xb18c,0xb158,0xb126,0xb0ff,0xb0c3,0xb094,0xb062,0xb02b,0xb001,0xafc3,0xaf93,0xaf5c,0xaf25,
0xaef6,0xaeb7,0xae88,0xae49,0xae14,0xadda,0xad9f,0xad69,0xad2a,0xacf7,0xacb3,0xac81,0xac3a,0xac06,0xabbf,0xab89,
0xab42,0xab0a,0xaac3,0xaa89,0xaa41,0xaa07,0xa9bd,0xa983,0xa936,0xa8fc,0xa8ae,0xa86f,0xa824,0xa7e0,0xa798,0xa74e,
0xa70c,0xa6bc,0xa67d,0xa629,0xa5e1,0xa596,0xa547,0xa503,0xa4ac,0xa462,0xa414,0xa3c0,0xa37a,0xa321,0xa2d0,0xa286,
0xa22b,0xa1da,0xa18a,0xa12f,0xa0dd,0xa08b,0xa02e,0x9fd9,0x9f87,0x9f27,0x9ece,0x9e7d,0x9e1b,0x9dbd,0x9d65,0x9d0b,
0x9ca7,0x9c49,0x9bf0,0x9b8f,0x9b2a,0x9ac9,0x9a6d,0x9a0b,0x99a3,0x993d,0x98dc,0x987e,0x9812,0x97a7,0x973f,0x96d9,
0x9638,0x9569,0x9490,0x93b0,0x92d3,0x91f9,0x9110,0x9024,0x8f38,0x8e4b,0x8d5b,0x8c69,0x8b73,0x8a79,0x897b,0x8879,
0x8771,0x8666,0x8558,0x8446,0x8333,0x821e,0x8109,0x803a,0x800c,0x8003,0x8001,0x8000,0x8000,0x8000,0x8000,0x8000,
0x8000,0x8000,0x8000,0x8000,0x8000,0x8000,0x8000,0x8000,0x8000,0x8000,0x8000,0x8000,0x8000,0x8000,0x8000,0x8000,
0x8000,0x8000,0x8000,0x8000,0x8000,0x8000,0x8000,0x8000,0x8000,0x8000,0x8000,0x8000,0x8000,0x8000,0x8000,0x8000,
0x8000,0x8000,0x8000,0x8000,0x8000,0x8000,0x8000,0x8000,0x8000,0x8000,0x8000,0x8000,0x8000,0x8000,0x8000,0x8000,
0x8000,0x8000,0x8000,0x8000,0x8000,0x8000,0x8000,0x8000,0x8000,0x8000,0x8000,0x8000,0x8000,0x8000,0x8000,0x8000,
0x8000,0x8000,0x8000,0x8000,0x8000,0x8000,0x8000,0x8000,0x8000,0x8000,0x8000,0x8000,0x8000,0x8000,0x8000,0x8000,
0x8000,0x8000,0x8000,0x8000,0x8000,0x8000,0x8000,0x8000,0x8000,0x8000,0x8000,0x8000,0x8000,0x8000,0x8000,0x8000,
0x8000,0x8000,0x8000,0x8000,0x8000,0x8000,0x8000,0x8000,0x8000,0x8000,0x8000,0x8000,0x8000,0x8000,0x8000,0x8000,
0x8000,0x8000,0x8000,0x8000,0x8000,0x8000,0x8000,0x8000,0x8000,0x8000,0x8000,0x8000,0x8000,0x8000,0x8000,0x8000,
0x8000,0x8000,0x8000,0x8000,0x8000,0x8000,0x8000,0x8000,0x8000,0x8000,0x8000,0x8000,0x8000,0x8000,0x8000,0x8000,
0x8000,0x8000,0x8000,0x8000,0x8000,0x8000,0x8000,0x8000,0x8000,0x8000,0x8000,0x8000,0x8000,0x8000,0x8000,0x8000,
0x8000,0x8000,0x8000,0x8000,0x8000,0x8000,0x8000,0x8000,0x8000,0x8000,0x8000,0x8000,0x8000,0x8000,0x8000,0x8000,
0x8000,0x8000,0x8000,0x8000,0x8000,0x8000,0x8000,0x8000,0x8000,0x8000,0x8000,0x8000,0x8000,0x8000,0x8000,0x8000,
0x8000,0x8000,0x8000,0x8000,0x8000,0x8000,0x8000,0x8000,0x8000,0x8000,0x8000,0x8000,0x8000,0x8000,0x8000,0x8000,
0x8000,0x8000,0x8000,0x8000,0x8000,0x8000,0x8000,0x8000,0x8000,0x8000,0x8000,0x8000,0x8000,0x8000,0x8000,0x8000,
0x8000,0x8000,0x8000,0x8000,0x8000,0x8000,0x8000,0x8000,0x8000,0x8000,0x8000,0x8000,0x8000,0x8000,0x8000,0x8000,
0x8000,0x8000,0x8000,0x8000,0x8000,0x8000,0x8000,0x8000,0x8000,0x8000,0x8000,0x8000,0x8000,0x8000,0x8000,0x8000,
0x8000,0x8000,0x8000,0x8000,0x8000,0x8000,0x8000,0x8000,0x8000,0x8000,0x8000,0x8000,0x8000,0x8000,0x8000,0x8000,
0x8000,0x8000,0x8000,0x8000,0x8000,0x8000,0x8000,0x8000,0x8000,0x8000,0x8000,0x8000,0x8000,0x8000,0x8000,0x8000,
0x8000,0x8000,0x8000,0x8000,0x8000,0x8000,0x8000,0x8000,0x8000,0x8000,0x8000,0x8000,0x8000,0x8000,0x8000,0x8000,
0x8000,0x8000,0x8000,0x8000,0x8000,0x8000,0x8000,0x8000,0x8000,0x8000,0x8000,0x8000,0x8000,0x8000,0x8000,0x8000,
0x8000,0x8000,0x8000,0x8000,0x8000,0x8000,0x8000,0x8000,0x8000,0x8000,0x8000,0x8000,0x8000,0x8000,0x8000,0x8000,
0x8000,0x8000,0x8000,0x8000,0x8000,0x8000,0x8000,0x8000,0x8000,0x8000,0x8000,0x8000,0x8000,0x8000,0x8000,0x8000
};

__global__ __launch_bounds__(512) void mixer_token_mlp_kernel(const void* __restrict__ x, const void* __restrict__ w1,
                                                             const void* __restrict__ b1, const void* __restrict__ w2,
                                                             const void* __restrict__ b2, void* __restrict__ out) {
  __shared__ __attribute__((aligned(1024))) char smem[163840];
  unsigned lds = (unsigned)(size_t)smem;
  unsigned bid = blockIdx.x;
  unsigned tid = threadIdx.x;
  const unsigned short* cells = gelu_cells;
  const __attribute__((address_space(4))) void* ka = (const __attribute__((address_space(4))) void*)__builtin_amdgcn_kernarg_segment_ptr();
  asm volatile(
    ".p2align 3\n"
    "s_setprio 3\n"
    "s_load_dwordx2 s[20:21], %0, 0x0\n"
    "s_load_dwordx2 s[24:25], %0, 0x8\n"
    "s_load_dwordx2 s[32:33], %0, 0x10\n"
    "s_load_dwordx2 s[28:29], %0, 0x18\n"
    "s_load_dwordx2 s[36:37], %0, 0x20\n"
    "s_load_dwordx2 s[40:41], %0, 0x28\n"
    "v_lshrrev_b32_e32 v7, 4, %2\n"
    "v_and_b32_e64 v6, 15, %2\n"
    "v_mul_u32_u24_e32 v8, 1152, v7\n"
    "s_and_b32 s46, %1, 7\n"
    "s_lshr_b32 s54, %1, 3\n"
    "v_lshl_add_u32 v9, v6, 4, v8\n"
    "s_lshl_b32 s46, s46, 2\n"
    "s_lshr_b32 s52, s54, 3\n"
    "v_lshl_add_u32 v10, v6, 3, v8\n"
    "s_add_u32 s46, s46, s52\n"
    "s_and_b32 s45, s54, 7\n"
    "v_and_b32_e64 v11, 1, v6\n"
    "s_mul_i32 s63, s46, 36864\n"
    "s_mul_i32 s53, s45, 144\n"
    "v_lshl_add_u32 v11, v11, 3, v8\n"
    "s_mov_b32 s22, 36864\n"
    "s_mov_b32 s23, 131072\n"
    "v_lshrrev_b32_e64 v12, 6, %2\n"
    "v_and_b32_e32 v5, 63, %2\n"
    "v_readfirstlane_b32 s44, v12\n"
    "v_bfe_u32 v12, v5, 4, 1\n"
    "v_bfe_u32 v13, v5, 2, 2\n"
    "v_lshrrev_b32_e32 v14, 5, v5\n"
    "v_and_b32_e32 v15, 3, v5\n"
    "v_lshlrev_b32_e64 v120, 4, v5\n"
    "s_mul_i32 s47, s44, 6144\n"
    "s_mul_i32 s52, s44, 0x02000200\n"
    "s_add_u32 s52, s52, 0x00020000\n"
    "v_mul_u32_u24_e32 v96, 0x80008, v5\n"
    "s_mov_b32 s88, 0x00040004\n"
    "s_mov_b32 s89, 0x00040004\n"
    "v_add_u32_e32 v96, s52, v96\n"
    "s_lshl_b32 s55, s44, 10\n"
    "s_add_u32 s55, s55, 65472\n"
    "v_add_u32_e32 v97, 0x00020002, v96\n"
    "v_add_u32_e64 v84, s55, v120\n"
    "v_lshl_add_u64 v[98:99], v[96:97], 0, s[88:89]\n"
    "v_add_u32_e32 v83, 65536, v84\n"
    "s_mov_b32 s52, 0x00800080\n"
    "s_mov_b32 s90, 0x80008000\n"
    "s_mov_b32 s91, 0x80008000\n"
    "v_pk_sub_u16 v100, v96, s52 clamp\n"
    "v_pk_sub_u16 v101, v97, s52 clamp\n"
    "v_pk_sub_u16 v102, v98, s52 clamp\n"
    "v_pk_sub_u16 v103, v99, s52 clamp\n"
    "ds_write_b128 v84, v[100:103]\n"
    "s_mov_b32 s26, 294912\n"
    "s_mov_b32 s27, 131072\n"
    "s_mul_i32 s48, s44, 1152\n"
    "v_lshl_add_u32 v85, v12, 3, v13\n"
    "v_lshl_add_u32 v85, v14, 2, v85\n"
    "v_mul_u32_u24_e32 v116, 9216, v85\n"
    "v_lshl_add_u64 v[92:93], v[100:101], 0, s[90:91]\n"
    "v_lshl_add_u64 v[94:95], v[102:103], 0, s[90:91]\n"
    "ds_write_b128 v83, v[92:95]\n"
    "v_lshl_add_u32 v116, v15, 4, v116\n"
    "v_add_u32_e32 v117, 146432, v116\n"
    "s_mov_b32 s34, 9216\n"
    "s_mov_b32 s35, 131072\n"
    "s_mov_b32 s30, 294912\n"
    "s_mov_b32 s31, 131072\n"
    "s_mov_b32 s88, 0x0f800f80\n"
    "s_mov_b32 s89, s88\n"
    "v_lshl_add_u64 v[88:89], v[96:97], 0, s[88:89]\n"
    "v_lshl_add_u64 v[90:91], v[98:99], 0, s[88:89]\n"
    "ds_write_b128 v84, v[88:91] offset:8192\n"
    "s_mul_i32 s49, s44, 36864\n"
    "s_mov_b32 s42, 36864\n"
    "s_mov_b32 s43, 131072\n"
    "s_mov_b32 s38, 64\n"
    "s_mov_b32 s39, 131072\n"
    "v_lshlrev_b32_e32 v38, 6, v13\n"
    "s_mov_b32 s90, 0x1f801f80\n"
    "s_mov_b32 s91, s90\n"
    "v_lshl_add_u64 v[100:101], v[96:97], 0, s[90:91]\n"
    "v_lshl_add_u64 v[102:103], v[98:99], 0, s[90:91]\n"
    "ds_write_b128 v84, v[100:103] offset:16384\n"
    "v_lshl_add_u32 v38, v12, 5, v38\n"
    "v_lshl_add_u32 v38, v15, 3, v38\n"
    "v_lshl_add_u32 v112, v14, 8, v38\n"
    "v_add_u32_e32 v112, s47, v112\n"
    "s_mul_i32 s60, s44, 1152\n"
    "s_add_u32 s60, s60, 55296\n"
    "s_waitcnt lgkmcnt(0)\n"
    "s_add_u32 s20, s20, s63\n"
    "s_addc_u32 s21, s21, 0\n"
    "buffer_load_dwordx4 v[16:19], v9, s[20:23], 0 offen\n"
    "buffer_load_dwordx4 v[20:23], v9, s[20:23], 0 offen offset:256\n"
    "buffer_load_dwordx4 v[24:27], v9, s[20:23], 0 offen offset:512\n"
    "buffer_load_dwordx4 v[28:31], v9, s[20:23], 0 offen offset:768\n"
    "buffer_load_dwordx2 v[32:33], v10, s[20:23], 0 offen offset:1024\n"
    "buffer_load_dwordx2 v[34:35], v10, s[20:23], s53 offen\n"
    "buffer_load_dwordx2 v[36:37], v11, s[20:23], s53 offen offset:128\n"
    "s_add_u32 s40, s40, s63\n"
    "s_addc_u32 s41, s41, 0\n"
    "s_setprio 0\n"
    "s_mov_b32 s88, 0x29802980\n"
    "s_mov_b32 s89, s88\n"
    "v_lshl_add_u64 v[92:93], v[96:97], 0, s[88:89]\n"
    "v_lshl_add_u64 v[94:95], v[98:99], 0, s[88:89]\n"
    "ds_write_b128 v84, v[92:95] offset:21504\n"
    "v_add_u32_e32 v38, s60, v38\n"
    "v_and_b32_e32 v113, 60, v5\n"
    "v_lshl_add_u32 v113, v113, 1, v15\n"
    "v_lshl_add_u32 v113, v113, 3, s47\n"
    "v_lshl_add_u32 v109, v14, 3, v13\n"
    "v_mul_u32_u24_e32 v109, 192, v109\n"
    "v_add_u32_e32 v109, 49152, v109\n"
    "v_lshl_add_u32 v108, v12, 5, v109\n"
    "v_lshl_add_u32 v108, v15, 3, v108\n"
    "s_mov_b32 s90, 0x42004200\n"
    "s_mov_b32 s91, s90\n"
    "v_lshl_add_u64 v[88:89], v[96:97], 0, s[90:91]\n"
    "v_lshl_add_u64 v[90:91], v[98:99], 0, s[90:91]\n"
    "ds_write_b128 v84, v[88:91] offset:33792\n"
    "v_lshrrev_b32_e32 v39, 1, v15\n"
    "v_cmp_eq_u32_e32 vcc, v12, v39\n"
    "v_and_b32_e32 v39, 1, v15\n"
    "v_lshlrev_b32_e32 v39, 3, v39\n"
    "v_cndmask_b32_e32 v39, 16, v39, vcc\n"
    "v_add_u32_e64 v109, v39, v109\n"
    "v_mul_u32_u24_e32 v110, 192, v7\n"
    "v_add_u32_e32 v110, 49152, v110\n"
    "v_min_u32_e32 v39, 2, v6\n"
    "s_mov_b32 s88, 0x52005200\n"
    "s_mov_b32 s89, s88\n"
    "v_lshl_add_u64 v[100:101], v[96:97], 0, s[88:89]\n"
    "v_lshl_add_u64 v[102:103], v[98:99], 0, s[88:89]\n"
    "ds_write_b128 v84, v[100:103] offset:41984\n"
    "v_lshl_add_u32 v111, v39, 3, v110\n"
    "v_lshl_add_u32 v110, v6, 3, v110\n"
    "v_cmp_gt_u32_e64 s[60:61], 2, v6\n"
    "s_mov_b32 s50, 0xc09d30fc\n"
    "s_mov_b32 s51, 0xc09d30fc\n"
    "s_mov_b32 s56, 0xffff0000\n"
    "s_mov_b32 s57, 0x3ae38e39\n"
    "s_mov_b32 s58, 0x3727c5ac\n"
    "s_mov_b32 s62, 0x3eeff558\n"
    "s_mov_b32 s86, 0x22222222\n"
    "s_mov_b32 s90, 0x62006200\n"
    "s_mov_b32 s91, s90\n"
    "v_lshl_add_u64 v[92:93], v[96:97], 0, s[90:91]\n"
    "v_lshl_add_u64 v[94:95], v[98:99], 0, s[90:91]\n"
    "ds_write_b128 v84, v[92:95] offset:50176\n"
    "s_mov_b32 s87, 0x22222222\n"
    "s_mov_b32 s92, 0x44444444\n"
    "s_mov_b32 s93, 0x44444444\n"
    "s_mov_b32 s100, 0x88888888\n"
    "s_mov_b32 s101, 0x88888888\n"
    "v_mov_b32_e32 v40, 0x0000b8ab\n"
    "v_mov_b32_e32 v41, 0x00003ef0\n"
    "v_cmp_gt_u32_e32 vcc, 32, v5\n"
    "v_bfe_u32 v42, v5, 3, 1\n"
    "s_mov_b32 s88, 0x70007000\n"
    "s_mov_b32 s89, s88\n"
    "v_lshl_add_u64 v[88:89], v[96:97], 0, s[88:89]\n"
    "v_lshl_add_u64 v[90:91], v[98:99], 0, s[88:89]\n"
    "ds_write_b128 v84, v[88:91] offset:57344\n"
    "v_mov_b32_e32 v44, 0x3f80\n"
    "v_cndmask_b32_e32 v40, v40, v41, vcc\n"
    "v_cmp_eq_u32_e32 vcc, v12, v42\n"
    "v_lshlrev_b32_e32 v41, 16, v40\n"
    "v_accvgpr_write_b32 a122, v44\n"
    "v_cndmask_b32_e64 v42, 0, v40, vcc\n"
    "v_cndmask_b32_e32 v43, 0, v41, vcc\n"
    "v_accvgpr_write_b32 a123, 0\n"
    "v_accvgpr_write_b32 a106, v40\n"
    "s_mov_b32 s90, 0x8f808f80\n"
    "s_mov_b32 s91, s90\n"
    "v_lshl_add_u64 v[100:101], v[96:97], 0, s[90:91]\n"
    "v_lshl_add_u64 v[102:103], v[98:99], 0, s[90:91]\n"
    "ds_write_b128 v83, v[100:103] offset:8192\n"
    "v_accvgpr_write_b32 a107, 0\n"
    "s_mov_b64 s[96:97], %4\n"
    "s_mov_b32 s98, 8192\n"
    "s_mov_b32 s99, 131072\n"
    "s_and_b32 s54, s44, 3\n"
    "s_lshl_b32 s54, s54, 10\n"
    "s_lshr_b32 s59, s44, 2\n"
    "s_lshl_b32 s59, s59, 16\n"
    "s_add_u32 s54, s54, s59\n"
    "s_add_u32 m0, s54, 95168\n"
    "s_lshl_b32 s59, s44, 10\n"
    "s_cmp_eq_u32 s44, 7\n"
    "s_cselect_b64 exec, 0xf, -1\n"
    "buffer_load_dwordx4 v120, s[96:99], s59 offen lds\n"
    "s_mov_b64 exec, -1\n"
    "s_mov_b32 m0, s47\n"
    "s_nop 0\n"
    "buffer_load_dwordx4 v116, s[24:27], s48 offen lds\n"
    "buffer_load_dwordx4 v117, s[24:27], s48 offen offset:1024 lds\n"
    "s_add_u32 s48, s48, 64\n"
    "s_mul_i32 s59, s44, 1152\n"
    "s_add_u32 m0, s59, 55296\n"
    "s_nop 0\n"
    "buffer_load_dwordx4 v120, s[32:35], s59 offen lds\n"
    "s_mov_b64 exec, 0xffff\n"
    "buffer_load_dwordx4 v120, s[32:35], s59 offen offset:1024 lds\n"
    "s_mov_b64 exec, -1\n"
    "v_accvgpr_write_b32 a108, v41\n"
    "v_accvgpr_write_b32 a109, 0\n"
    "v_accvgpr_write_b32 a110, 0\n"
    "v_accvgpr_write_b32 a111, v40\n"
    "v_accvgpr_write_b32 a112, 0\n"
    "v_accvgpr_write_b32 a113, v41\n"
    "v_accvgpr_write_b32 a114, v42\n"
    "v_accvgpr_write_b32 a115, 0\n"
    "v_accvgpr_write_b32 a116, v43\n"
    "s_mov_b32 s88, 0x9f809f80\n"
    "s_mov_b32 s89, s88\n"
    "v_lshl_add_u64 v[92:93], v[96:97], 0, s[88:89]\n"
    "v_lshl_add_u64 v[94:95], v[98:99], 0, s[88:89]\n"
    "ds_write_b128 v83, v[92:95] offset:16384\n"
    "v_accvgpr_write_b32 a117, 0\n"
    "v_accvgpr_write_b32 a118, 0\n"
    "v_accvgpr_write_b32 a119, v42\n"
    "v_accvgpr_write_b32 a120, 0\n"
    "v_accvgpr_write_b32 a121, v43\n"
    "v_mov_b32_e32 v40, 0x3f803f80\n"
    "v_mov_b32_e32 v41, v40\n"
    "v_mov_b32_e32 v106, s44\n"
    "v_lshlrev_b32_e32 v107, 6, v5\n"
    "s_mov_b32 s90, 0xa980a980\n"
    "s_mov_b32 s91, s90\n"
    "v_lshl_add_u64 v[88:89], v[96:97], 0, s[90:91]\n"
    "v_lshl_add_u64 v[90:91], v[98:99], 0, s[90:91]\n"
    "ds_write_b128 v83, v[88:91] offset:21504\n"
    "v_and_b32_e64 v106, 3, v106\n"
    "v_lshl_add_u32 v107, v106, 12, v107\n"
    "v_add_u32_e32 v106, 1, v106\n"
    "v_lshrrev_b32_e32 v106, 2, v106\n"
    "v_lshl_add_u32 v107, v106, 30, v107\n"
    "v_mov_b32_e32 v106, %1\n"
    "v_lshrrev_b32_e32 v106, 3, v106\n"
    "v_mul_u32_u24_e32 v106, 9216, v106\n"
    "v_add_u32_e32 v107, v106, v107\n"
    "s_waitcnt vmcnt(11)\n"
    "v_mfma_f32_4x4x4bf16_1k v[44:47], v[16:17], v[40:41], 0\n"
    "v_mfma_f32_4x4x4bf16_1k v[48:51], v[16:17], v[16:17], 0\n"
    "s_nop 0\n"
    "v_mfma_f32_4x4x4bf16_1k v[44:47], v[18:19], v[40:41], v[44:47]\n"
    "v_mfma_f32_4x4x4bf16_1k v[48:51], v[18:19], v[18:19], v[48:51]\n"
    "s_waitcnt vmcnt(6)\n"
    "v_lshlrev_b32_e64 v86, 16, v34\n"
    "v_mfma_f32_4x4x4bf16_1k v[44:47], v[20:21], v[40:41], v[44:47]\n"
    "v_mfma_f32_4x4x4bf16_1k v[48:51], v[20:21], v[20:21], v[48:51]\n"
    "v_and_b32_e64 v87, s56, v34\n"
    "v_mfma_f32_4x4x4bf16_1k v[44:47], v[22:23], v[40:41], v[44:47]\n"
    "v_mfma_f32_4x4x4bf16_1k v[48:51], v[22:23], v[22:23], v[48:51]\n"
    "v_lshlrev_b32_e64 v88, 16, v35\n"
    "v_mfma_f32_4x4x4bf16_1k v[44:47], v[24:25], v[40:41], v[44:47]\n"
    "v_mfma_f32_4x4x4bf16_1k v[48:51], v[24:25], v[24:25], v[48:51]\n"
    "v_and_b32_e64 v89, s56, v35\n"
    "v_mfma_f32_4x4x4bf16_1k v[44:47], v[26:27], v[40:41], v[44:47]\n"
    "v_mfma_f32_4x4x4bf16_1k v[48:51], v[26:27], v[26:27], v[48:51]\n"
    "s_waitcnt vmcnt(5)\n"
    "v_lshlrev_b32_e32 v90, 16, v36\n"
    "v_mfma_f32_4x4x4bf16_1k v[44:47], v[28:29], v[40:41], v[44:47]\n"
    "v_mfma_f32_4x4x4bf16_1k v[48:51], v[28:29], v[28:29], v[48:51]\n"
    "v_and_b32_e64 v91, s56, v36\n"
    "v_mfma_f32_4x4x4bf16_1k v[44:47], v[30:31], v[40:41], v[44:47]\n"
    "v_mfma_f32_4x4x4bf16_1k v[48:51], v[30:31], v[30:31], v[48:51]\n"
    "v_lshlrev_b32_e64 v92, 16, v37\n"
    "v_mfma_f32_4x4x4bf16_1k v[44:47], v[32:33], v[40:41], v[44:47]\n"
    "v_mfma_f32_4x4x4bf16_1k v[48:51], v[32:33], v[32:33], v[48:51]\n"
    "v_and_b32_e32 v93, s56, v37\n"
    "s_nop 2\n"
    "v_pk_add_f32 v[44:45], v[44:45], v[46:47]\n"
    "v_cndmask_b32_e64 v48, v48, v49, s[86:87]\n"
    "v_add_f32_e64 v44, v44, v45\n"
    "v_cndmask_b32_e64 v48, v48, v50, s[92:93]\n"
    "v_cndmask_b32_e64 v48, v48, v51, s[100:101]\n"
    "v_add_f32_dpp v44, v44, v44 row_ror:4 row_mask:0xf bank_mask:0xf\n"
    "s_nop 0\n"
    "v_add_f32_dpp v48, v48, v48 row_ror:1 row_mask:0xf bank_mask:0xf\n"
    "v_add_f32_dpp v44, v44, v44 row_ror:8 row_mask:0xf bank_mask:0xf\n"
    "s_nop 0\n"
    "v_add_f32_dpp v48, v48, v48 row_ror:2 row_mask:0xf bank_mask:0xf\n"
    "v_mul_f32_e32 v80, s57, v44\n"
    "s_nop 0\n"
    "v_add_f32_dpp v48, v48, v48 row_ror:4 row_mask:0xf bank_mask:0xf\n"
    "v_mul_f32_e32 v83, v80, v80\n"
    "s_nop 0\n"
    "v_add_f32_dpp v48, v48, v48 row_ror:8 row_mask:0xf bank_mask:0xf\n"
    "v_mul_f32_e32 v84, 0x3c800000, v83\n"
    "v_fma_f32 v82, v48, s57, -v83\n"
    "v_add_f32_e32 v82, s58, v82\n"
    "v_cmp_lt_f32_e32 vcc, v82, v84\n"
    "v_rsq_f32_e32 v82, v82\n"
    "s_nop 0\n"
    "v_mul_f32_e32 v82, s62, v82\n"
    "v_mul_f32_e64 v80, -v80, v82\n"
    "s_cbranch_vccnz Lfb%=\n"
    "Lret%=:\n"
    "v_pk_fma_f32 v[86:87], v[86:87], v[82:83], v[80:81] op_sel_hi:[1,0,0]\n"
    "v_pk_fma_f32 v[88:89], v[88:89], v[82:83], v[80:81] op_sel_hi:[1,0,0]\n"
    "v_pk_fma_f32 v[90:91], v[90:91], v[82:83], v[80:81] op_sel_hi:[1,0,0]\n"
    "v_pk_fma_f32 v[92:93], v[92:93], v[82:83], v[80:81] op_sel_hi:[1,0,0]\n"
    "v_cvt_pk_bf16_f32 v100, v86, v87\n"
    "v_cvt_pk_bf16_f32 v101, v88, v89\n"
    "v_cvt_pk_bf16_f32 v102, v90, v91\n"
    "v_cvt_pk_bf16_f32 v103, v92, v93\n"
    "ds_write_b64 v110, v[100:101]\n"
    "v_cndmask_b32_e64 v102, 0, v102, s[60:61]\n"
    "v_cndmask_b32_e64 v103, 0, v103, s[60:61]\n"
    "ds_write_b64 v111, v[102:103] offset:128\n"
    "s_add_u32 m0, s47, 2048\n"
    "s_nop 0\n"
    "buffer_load_dwordx4 v116, s[24:27], s48 offen lds\n"
    "buffer_load_dwordx4 v117, s[24:27], s48 offen offset:1024 lds\n"
    "s_add_u32 s48, s48, 64\n"
    "s_add_u32 m0, s47, 4096\n"
    "s_nop 0\n"
    "buffer_load_dwordx4 v120, s[28:31], s49 offen lds\n"
    "buffer_load_dwordx4 v120, s[28:31], s49 offen offset:1024 lds\n"
    "s_add_u32 s49, s49, 2048\n"
    "s_waitcnt vmcnt(4)\n"
    "ds_read_b64_tr_b16 a[96:97], v38\n"
    "ds_read_b64_tr_b16 a[98:99], v38 offset:256\n"
    "ds_read_b64_tr_b16 a[100:101], v38 offset:512\n"
    "ds_read_b64_tr_b16 a[102:103], v38 offset:768\n"
    "ds_read_b64_tr_b16 a[104:105], v38 offset:1024\n"
    "ds_read_b64_tr_b16 a[72:73], v112\n"
    "ds_read_b64_tr_b16 a[74:75], v112 offset:512\n"
    "ds_read_b64_tr_b16 a[76:77], v112 offset:1024\n"
    "ds_read_b64_tr_b16 a[78:79], v112 offset:1536\n"
    "s_waitcnt lgkmcnt(8)\n"
    "v_mfma_f32_32x32x8bf16_1k v[16:31], a[96:97], a[106:107], 0\n"
    "v_mfma_f32_16x16x16bf16_1k v[96:99], a[96:97], a[114:115], 0\n"
    "s_mov_b32 m0, s47\n"
    "s_waitcnt lgkmcnt(0)\n"
    "buffer_load_dwordx4 v116, s[24:27], s48 offen lds\n"
    "buffer_load_dwordx4 v117, s[24:27], s48 offen offset:1024 lds\n"
    "s_add_u32 s48, s48, 64\n"
    "s_waitcnt lgkmcnt(0)\n"
    "s_barrier\n"
    "s_cmp_lt_u32 s44, 4\n"
    "s_cselect_b32 s96, s24, s28\n"
    "s_cselect_b32 s97, s25, s29\n"
    "s_mov_b32 s98, 294912\n"
    "s_mov_b32 s99, 131072\n"
    "buffer_load_dword v124, v107, s[96:99], 0 offen\n"
    "ds_read_b64_tr_b16 a[56:57], v108 offset:64\n"
    "ds_read_b64_tr_b16 a[58:59], v108 offset:832\n"
    "ds_read_b64_tr_b16 a[48:49], v108\n"
    "ds_read_b64_tr_b16 a[50:51], v108 offset:768\n"
    "ds_read_b64_tr_b16 a[60:61], v108 offset:3136\n"
    "ds_read_b64_tr_b16 a[62:63], v108 offset:3904\n"
    "ds_read_b64_tr_b16 a[52:53], v108 offset:3072\n"
    "ds_read_b64_tr_b16 a[54:55], v108 offset:3840\n"
    "ds_read_b64_tr_b16 a[64:65], v109 offset:128\n"
    "ds_read_b64_tr_b16 a[66:67], v109 offset:896\n"
    "ds_read_b64_tr_b16 a[68:69], v109 offset:3200\n"
    "ds_read_b64_tr_b16 a[70:71], v109 offset:3968\n"
    "s_waitcnt lgkmcnt(10)\n"
    "v_mfma_f32_32x32x16_bf16 v[48:63], a[72:75], a[56:59], v[16:31]\n"
    "s_waitcnt lgkmcnt(8)\n"
    "v_mfma_f32_32x32x16_bf16 v[16:31], a[72:75], a[48:51], v[16:31]\n"
    "s_waitcnt lgkmcnt(4)\n"
    "v_mfma_f32_32x32x16_bf16 v[16:31], a[76:79], a[52:55], v[16:31]\n"
    "v_mfma_f32_32x32x16_bf16 v[48:63], a[76:79], a[60:63], v[48:63]\n"
    "s_waitcnt lgkmcnt(2)\n"
    "v_mfma_f32_16x16x32_bf16 v[96:99], a[72:75], a[64:67], v[96:99]\n"
    "s_waitcnt lgkmcnt(0)\n"
    "v_mfma_f32_16x16x32_bf16 v[96:99], a[76:79], a[68:71], v[96:99]\n"
    ".p2align 6\n"
    "s_cmp_lt_u32 s44, 4\n"
    "s_cbranch_scc1 Lp0%=\n"
    "s_setprio 1\n"
    "Lp0%=:\n"
    "s_cmp_lt_u32 s44, 4\n"
    "s_cbranch_scc0 Lq0%=\n"
    "s_setprio 2\n"
    "Lq0%=:\n"
    "s_nop 0\n"
    "v_pk_fma_f32 v[80:81], v[16:17], v[16:17], s[50:51] neg_lo:[0,1,0] neg_hi:[0,1,0]\n"
    "v_pk_fma_f32 v[82:83], v[18:19], v[18:19], s[50:51] neg_lo:[0,1,0] neg_hi:[0,1,0]\n"
    "v_pk_fma_f32 v[84:85], v[20:21], v[20:21], s[50:51] neg_lo:[0,1,0] neg_hi:[0,1,0]\n"
    "v_pk_fma_f32 v[86:87], v[22:23], v[22:23], s[50:51] neg_lo:[0,1,0] neg_hi:[0,1,0]\n"
    "v_pk_fma_f32 v[88:89], v[24:25], v[24:25], s[50:51] neg_lo:[0,1,0] neg_hi:[0,1,0]\n"
    "v_pk_fma_f32 v[90:91], v[26:27], v[26:27], s[50:51] neg_lo:[0,1,0] neg_hi:[0,1,0]\n"
    "v_pk_fma_f32 v[92:93], v[28:29], v[28:29], s[50:51] neg_lo:[0,1,0] neg_hi:[0,1,0]\n"
    "v_pk_mul_f32 v[80:81], v[80:81], v[16:17]\n"
    "v_pk_mul_f32 v[82:83], v[82:83], v[18:19]\n"
    "v_pk_mul_f32 v[84:85], v[84:85], v[20:21]\n"
    "v_pk_mul_f32 v[86:87], v[86:87], v[22:23]\n"
    "v_pk_mul_f32 v[88:89], v[88:89], v[24:25]\n"
    "v_pk_mul_f32 v[90:91], v[90:91], v[26:27]\n"
    "v_pk_mul_f32 v[92:93], v[92:93], v[28:29]\n"
    "v_exp_f32_e32 v80, v80\n"
    "v_exp_f32_e32 v81, v81\n"
    "v_exp_f32_e32 v82, v82\n"
    "v_exp_f32_e32 v83, v83\n"
    "v_exp_f32_e32 v84, v84\n"
    "v_exp_f32_e32 v85, v85\n"
    "s_waitcnt vmcnt(5)\n"
    "ds_read_b64_tr_b16 a[72:73], v112 offset:2048\n"
    "v_exp_f32_e32 v86, v86\n"
    "ds_read_b64_tr_b16 a[74:75], v112 offset:2560\n"
    "v_exp_f32_e32 v87, v87\n"
    "ds_read_b64_tr_b16 a[76:77], v112 offset:3072\n"
    "v_exp_f32_e64 v88, v88\n"
    "v_mfma_f32_16x16x16bf16_1k v[100:103], a[96:97], a[116:117], 0\n"
    "ds_read_b64_tr_b16 a[78:79], v112 offset:3584\n"
    "v_exp_f32_e32 v89, v89\n"
    "v_mfma_f32_32x32x8bf16_1k v[32:47], a[96:97], a[108:109], 0\n"
    "v_exp_f32_e32 v90, v90\n"
    "v_exp_f32_e32 v91, v91\n"
    "v_exp_f32_e32 v92, v92\n"
    "v_exp_f32_e32 v93, v93\n"
    "s_nop 2\n"
    "v_mfma_f32_16x16x4bf16_1k v[80:95], a[122:123], a[122:123], v[80:95]\n"
    "v_lshlrev_b32_sdwa v96, 1, v96 dst_sel:DWORD dst_unused:UNUSED_PAD src0_sel:DWORD src1_sel:WORD_1\n"
    "ds_read_u16 v96, v96 offset:65472\n"
    "v_lshlrev_b32_sdwa v97, 1, v97 dst_sel:DWORD dst_unused:UNUSED_PAD src0_sel:DWORD src1_sel:WORD_1\n"
    "v_lshlrev_b32_sdwa v98, 1, v98 dst_sel:DWORD dst_unused:UNUSED_PAD src0_sel:DWORD src1_sel:WORD_1\n"
    "v_lshlrev_b32_sdwa v99, 1, v99 dst_sel:DWORD dst_unused:UNUSED_PAD src0_sel:DWORD src1_sel:WORD_1\n"
    "v_lshlrev_b32_sdwa v30, 1, v30 dst_sel:DWORD dst_unused:UNUSED_PAD src0_sel:DWORD src1_sel:WORD_1\n"
    "v_lshlrev_b32_sdwa v31, 1, v31 dst_sel:DWORD dst_unused:UNUSED_PAD src0_sel:DWORD src1_sel:WORD_1\n"
    "s_add_u32 m0, s47, 2048\n"
    "s_waitcnt lgkmcnt(1)\n"
    "buffer_load_dwordx4 v120, s[28:31], s49 offen lds\n"
    "buffer_load_dwordx4 v120, s[28:31], s49 offen offset:1024 lds\n"
    "s_add_u32 s49, s49, 2048\n"
    "v_lshlrev_b32_sdwa v63, 1, v63 dst_sel:DWORD dst_unused:UNUSED_PAD src0_sel:DWORD src1_sel:WORD_1\n"
    "ds_read_u16_d16_hi v97, v97 offset:65472\n"
    "v_lshlrev_b32_sdwa v62, 1, v62 dst_sel:DWORD dst_unused:UNUSED_PAD src0_sel:DWORD src1_sel:WORD_1\n"
    "v_lshlrev_b32_sdwa v61, 1, v61 dst_sel:DWORD dst_unused:UNUSED_PAD src0_sel:DWORD src1_sel:WORD_1\n"
    "v_lshlrev_b32_sdwa v60, 1, v60 dst_sel:DWORD dst_unused:UNUSED_PAD src0_sel:DWORD src1_sel:WORD_1\n"
    "v_lshlrev_b32_sdwa v59, 1, v59 dst_sel:DWORD dst_unused:UNUSED_PAD src0_sel:DWORD src1_sel:WORD_1\n"
    "v_lshlrev_b32_sdwa v58, 1, v58 dst_sel:DWORD dst_unused:UNUSED_PAD src0_sel:DWORD src1_sel:WORD_1\n"
    "v_lshlrev_b32_sdwa v57, 1, v57 dst_sel:DWORD dst_unused:UNUSED_PAD src0_sel:DWORD src1_sel:WORD_1\n"
    "ds_read_u16 v98, v98 offset:65472\n"
    "v_mfma_f32_32x32x16_bf16 v[64:79], a[72:75], a[56:59], v[32:47]\n"
    "v_lshlrev_b32_sdwa v56, 1, v56 dst_sel:DWORD dst_unused:UNUSED_PAD src0_sel:DWORD src1_sel:WORD_1\n"
    "v_lshlrev_b32_sdwa v55, 1, v55 dst_sel:DWORD dst_unused:UNUSED_PAD src0_sel:DWORD src1_sel:WORD_1\n"
    "v_lshlrev_b32_sdwa v54, 1, v54 dst_sel:DWORD dst_unused:UNUSED_PAD src0_sel:DWORD src1_sel:WORD_1\n"
    "s_waitcnt vmcnt(5)\n"
    "ds_read_b64_tr_b16 a[80:81], v112 offset:4096\n"
    "v_lshlrev_b32_sdwa v53, 1, v53 dst_sel:DWORD dst_unused:UNUSED_PAD src0_sel:DWORD src1_sel:WORD_1\n"
    "v_mfma_f32_32x32x16_bf16 v[32:47], a[72:75], a[48:51], v[32:47]\n"
    "ds_read_u16_d16_hi v99, v99 offset:65472\n"
    "ds_read_b64_tr_b16 a[82:83], v112 offset:4608\n"
    "v_lshlrev_b32_sdwa v52, 1, v52 dst_sel:DWORD dst_unused:UNUSED_PAD src0_sel:DWORD src1_sel:WORD_1\n"
    "ds_read_b64_tr_b16 a[84:85], v112 offset:5120\n"
    "v_lshlrev_b32_sdwa v51, 1, v51 dst_sel:DWORD dst_unused:UNUSED_PAD src0_sel:DWORD src1_sel:WORD_1\n"
    "ds_read_b64_tr_b16 a[86:87], v112 offset:5632\n"
    "v_lshlrev_b32_sdwa v50, 1, v50 dst_sel:DWORD dst_unused:UNUSED_PAD src0_sel:DWORD src1_sel:WORD_1\n"
    "ds_read_b64_tr_b16 a[88:89], v113 offset:4096\n"
    "v_lshlrev_b32_sdwa v49, 1, v49 dst_sel:DWORD dst_unused:UNUSED_PAD src0_sel:DWORD src1_sel:WORD_1\n"
    "v_mfma_f32_32x32x16_bf16 v[32:47], a[76:79], a[52:55], v[32:47]\n"
    "ds_read_u16 v30, v30 offset:65472\n"
    "ds_read_b64_tr_b16 a[92:93], v113 offset:4128\n"
    "v_lshlrev_b32_sdwa v48, 1, v48 dst_sel:DWORD dst_unused:UNUSED_PAD src0_sel:DWORD src1_sel:WORD_1\n"
    "ds_read_b64_tr_b16 a[124:125], v113 offset:5120\n"
    "ds_read_b64_tr_b16 v[4:5], v113 offset:5152\n"
    "v_rcp_f32_e32 v80, v80\n"
    "v_rcp_f32_e32 v81, v81\n"
    "v_mfma_f32_32x32x16_bf16 v[64:79], a[76:79], a[60:63], v[64:79]\n"
    "ds_read_u16_d16_hi v31, v31 offset:65472\n"
    "v_rcp_f32_e32 v82, v82\n"
    "v_rcp_f32_e32 v83, v83\n"
    "v_rcp_f32_e64 v84, v84\n"
    "ds_read_u16_d16_hi v63, v63 offset:65472\n"
    "v_mfma_f32_16x16x32_bf16 v[100:103], a[72:75], a[64:67], v[100:103]\n"
    "v_rcp_f32_e32 v85, v85\n"
    "v_mfma_f32_16x16x32_bf16 v[100:103], a[76:79], a[68:71], v[100:103]\n"
    "v_rcp_f32_e32 v86, v86\n"
    "ds_read_u16 v62, v62 offset:65472\n"
    "v_rcp_f32_e32 v87, v87\n"
    "v_rcp_f32_e32 v88, v88\n"
    "v_rcp_f32_e32 v89, v89\n"
    "ds_read_u16_d16_hi v61, v61 offset:65472\n"
    "v_rcp_f32_e32 v90, v90\n"
    "v_rcp_f32_e32 v91, v91\n"
    "v_rcp_f32_e32 v92, v92\n"
    "ds_read_u16 v60, v60 offset:65472\n"
    "v_rcp_f32_e64 v93, v93\n"
    "v_pk_mul_f32 v[16:17], v[16:17], v[80:81]\n"
    "v_pk_mul_f32 v[18:19], v[18:19], v[82:83]\n"
    "v_pk_mul_f32 v[20:21], v[20:21], v[84:85]\n"
    "ds_read_u16_d16_hi v59, v59 offset:65472\n"
    "v_pk_mul_f32 v[22:23], v[22:23], v[86:87]\n"
    "s_add_u32 m0, s47, 4096\n"
    "v_pk_mul_f32 v[24:25], v[24:25], v[88:89]\n"
    "v_pk_mul_f32 v[26:27], v[26:27], v[90:91]\n"
    "s_waitcnt lgkmcnt(6)\n"
    "buffer_load_dwordx4 v116, s[24:27], s48 offen lds\n"
    "v_pk_mul_f32 v[28:29], v[28:29], v[92:93]\n"
    "buffer_load_dwordx4 v117, s[24:27], s48 offen offset:1024 lds\n"
    "s_add_u32 s48, s48, 64\n"
    "v_pk_fma_f32 v[80:81], v[32:33], v[32:33], s[50:51] neg_lo:[0,1,0] neg_hi:[0,1,0]\n"
    "v_pk_fma_f32 v[82:83], v[34:35], v[34:35], s[50:51] neg_lo:[0,1,0] neg_hi:[0,1,0]\n"
    "ds_read_u16 v58, v58 offset:65472\n"
    "v_pk_fma_f32 v[84:85], v[36:37], v[36:37], s[50:51] neg_lo:[0,1,0] neg_hi:[0,1,0]\n"
    "v_pk_fma_f32 v[86:87], v[38:39], v[38:39], s[50:51] neg_lo:[0,1,0] neg_hi:[0,1,0]\n"
    "v_pk_fma_f32 v[88:89], v[40:41], v[40:41], s[50:51] neg_lo:[0,1,0] neg_hi:[0,1,0]\n"
    "v_pk_fma_f32 v[90:91], v[42:43], v[42:43], s[50:51] neg_lo:[0,1,0] neg_hi:[0,1,0]\n"
    "v_pk_fma_f32 v[92:93], v[44:45], v[44:45], s[50:51] neg_lo:[0,1,0] neg_hi:[0,1,0]\n"
    "ds_read_u16_d16_hi v57, v57 offset:65472\n"
    "v_pk_mul_f32 v[80:81], v[80:81], v[32:33]\n"
    "v_pk_mul_f32 v[82:83], v[82:83], v[34:35]\n"
    "v_pk_mul_f32 v[84:85], v[84:85], v[36:37]\n"
    "v_pk_mul_f32 v[86:87], v[86:87], v[38:39]\n"
    "v_pk_mul_f32 v[88:89], v[88:89], v[40:41]\n"
    "v_pk_mul_f32 v[90:91], v[90:91], v[42:43]\n"
    "ds_read_u16 v56, v56 offset:65472\n"
    "v_pk_mul_f32 v[92:93], v[92:93], v[44:45]\n"
    "v_cvt_pk_bf16_f32 v16, v16, v17\n"
    "v_cvt_pk_bf16_f32 v17, v18, v19\n"
    "v_cvt_pk_bf16_f32 v18, v20, v21\n"
    "v_cvt_pk_bf16_f32 v19, v22, v23\n"
    "v_cvt_pk_bf16_f32 v20, v24, v25\n"
    "ds_read_u16_d16_hi v55, v55 offset:65472\n"
    "v_cvt_pk_bf16_f32 v21, v26, v27\n"
    "v_cvt_pk_bf16_f32 v22, v28, v29\n"
    "s_waitcnt lgkmcnt(9)\n"
    "v_or_b32_e32 v23, v30, v31\n"
    "v_or_b32_e32 v108, v96, v97\n"
    "v_or_b32_e32 v109, v98, v99\n"
    "v_mfma_f32_32x32x16_bf16 a[0:15], v[16:19], a[80:83], 0\n"
    "ds_read_u16 v54, v54 offset:65472\n"
    "v_exp_f32_e32 v80, v80\n"
    "v_exp_f32_e32 v81, v81\n"
    "v_mfma_f32_32x32x16_bf16 a[0:15], v[20:23], a[84:87], a[0:15]\n"
    "ds_read_u16_d16_hi v53, v53 offset:65472\n"
    "v_exp_f32_e32 v82, v82\n"
    "v_exp_f32_e32 v83, v83\n"
    "v_exp_f32_e64 v84, v84\n"
    "ds_read_u16 v52, v52 offset:65472\n"
    "v_exp_f32_e32 v85, v85\n"
    "s_waitcnt vmcnt(5)\n"
    "ds_read_b64_tr_b16 a[72:73], v112\n"
    "v_exp_f32_e32 v86, v86\n"
    "ds_read_b64_tr_b16 a[74:75], v112 offset:512\n"
    "v_exp_f32_e32 v87, v87\n"
    "ds_read_u16_d16_hi v51, v51 offset:65472\n"
    "ds_read_b64_tr_b16 a[76:77], v112 offset:1024\n"
    "v_exp_f32_e64 v88, v88\n"
    "v_mfma_f32_16x16x16bf16_1k v[96:99], a[96:97], a[118:119], 0\n"
    "ds_read_b64_tr_b16 a[78:79], v112 offset:1536\n"
    "v_exp_f32_e64 v89, v89\n"
    "v_mfma_f32_32x32x8bf16_1k v[16:31], a[96:97], a[110:111], 0\n"
    "ds_read_u16 v50, v50 offset:65472\n"
    "v_exp_f32_e32 v90, v90\n"
    "v_exp_f32_e32 v91, v91\n"
    "v_exp_f32_e64 v92, v92\n"
    "ds_read_u16_d16_hi v49, v49 offset:65472\n"
    "v_exp_f32_e32 v93, v93\n"
    "s_nop 2\n"
    "v_mfma_f32_16x16x4bf16_1k v[80:95], a[122:123], a[122:123], v[80:95]\n"
    "v_lshlrev_b32_sdwa v100, 1, v100 dst_sel:DWORD dst_unused:UNUSED_PAD src0_sel:DWORD src1_sel:WORD_1\n"
    "s_waitcnt lgkmcnt(15)\n"
    "v_or_b32_e32 v63, v62, v63\n"
    "ds_read_u16 v48, v48 offset:65472\n"
    "v_lshlrev_b32_sdwa v101, 1, v101 dst_sel:DWORD dst_unused:UNUSED_PAD src0_sel:DWORD src1_sel:WORD_1\n"
    "v_or_b32_e32 v62, v60, v61\n"
    "v_lshlrev_b32_sdwa v102, 1, v102 dst_sel:DWORD dst_unused:UNUSED_PAD src0_sel:DWORD src1_sel:WORD_1\n"
    "s_mov_b32 m0, s47\n"
    "s_waitcnt lgkmcnt(14)\n"
    "v_or_b32_e32 v61, v58, v59\n"
    "v_lshlrev_b32_sdwa v103, 1, v103 dst_sel:DWORD dst_unused:UNUSED_PAD src0_sel:DWORD src1_sel:WORD_1\n"
    "s_waitcnt lgkmcnt(12)\n"
    "v_or_b32_e32 v60, v56, v57\n"
    "ds_read_u16 v100, v100 offset:65472\n"
    "v_lshlrev_b32_sdwa v46, 1, v46 dst_sel:DWORD dst_unused:UNUSED_PAD src0_sel:DWORD src1_sel:WORD_1\n"
    "s_waitcnt lgkmcnt(4)\n"
    "buffer_load_dwordx4 v120, s[28:31], s49 offen lds\n"
    "v_or_b32_e32 v59, v54, v55\n"
    "v_lshlrev_b32_sdwa v47, 1, v47 dst_sel:DWORD dst_unused:UNUSED_PAD src0_sel:DWORD src1_sel:WORD_1\n"
    "v_or_b32_e32 v58, v52, v53\n"
    "s_waitcnt lgkmcnt(3)\n"
    "v_or_b32_e64 v57, v50, v51\n"
    "buffer_load_dwordx4 v120, s[28:31], s49 offen offset:1024 lds\n"
    "s_waitcnt lgkmcnt(1)\n"
    "v_or_b32_e32 v56, v48, v49\n"
    "ds_read_u16_d16_hi v101, v101 offset:65472\n"
    "v_mfma_f32_32x32x16_bf16 a[16:31], v[60:63], a[84:87], 0\n"
    "s_add_u32 s49, s49, 2048\n"
    "v_lshlrev_b32_sdwa v79, 1, v79 dst_sel:DWORD dst_unused:UNUSED_PAD src0_sel:DWORD src1_sel:WORD_1\n"
    "v_lshlrev_b32_sdwa v78, 1, v78 dst_sel:DWORD dst_unused:UNUSED_PAD src0_sel:DWORD src1_sel:WORD_1\n"
    "v_lshlrev_b32_sdwa v77, 1, v77 dst_sel:DWORD dst_unused:UNUSED_PAD src0_sel:DWORD src1_sel:WORD_1\n"
    "v_mfma_f32_32x32x16_bf16 a[16:31], v[56:59], a[80:83], a[16:31]\n"
    "ds_read_u16 v102, v102 offset:65472\n"
    "v_lshlrev_b32_sdwa v76, 1, v76 dst_sel:DWORD dst_unused:UNUSED_PAD src0_sel:DWORD src1_sel:WORD_1\n"
    "v_lshlrev_b32_sdwa v75, 1, v75 dst_sel:DWORD dst_unused:UNUSED_PAD src0_sel:DWORD src1_sel:WORD_1\n"
    "v_lshlrev_b32_sdwa v74, 1, v74 dst_sel:DWORD dst_unused:UNUSED_PAD src0_sel:DWORD src1_sel:WORD_1\n"
    "v_lshlrev_b32_sdwa v73, 1, v73 dst_sel:DWORD dst_unused:UNUSED_PAD src0_sel:DWORD src1_sel:WORD_1\n"
    "v_mfma_f32_32x32x16_bf16 v[48:63], a[72:75], a[56:59], v[16:31]\n"
    "v_lshlrev_b32_sdwa v72, 1, v72 dst_sel:DWORD dst_unused:UNUSED_PAD src0_sel:DWORD src1_sel:WORD_1\n"
    "ds_read_u16_d16_hi v103, v103 offset:65472\n"
    "v_lshlrev_b32_sdwa v71, 1, v71 dst_sel:DWORD dst_unused:UNUSED_PAD src0_sel:DWORD src1_sel:WORD_1\n"
    "v_lshlrev_b32_sdwa v70, 1, v70 dst_sel:DWORD dst_unused:UNUSED_PAD src0_sel:DWORD src1_sel:WORD_1\n"
    "s_waitcnt vmcnt(4)\n"
    "ds_read_b64_tr_b16 a[80:81], v112 offset:2048\n"
    "v_lshlrev_b32_sdwa v69, 1, v69 dst_sel:DWORD dst_unused:UNUSED_PAD src0_sel:DWORD src1_sel:WORD_1\n"
    "v_mfma_f32_32x32x16_bf16 v[16:31], a[72:75], a[48:51], v[16:31]\n"
    "ds_read_b64_tr_b16 a[82:83], v112 offset:2560\n"
    "v_lshlrev_b32_sdwa v68, 1, v68 dst_sel:DWORD dst_unused:UNUSED_PAD src0_sel:DWORD src1_sel:WORD_1\n"
    "ds_read_b64_tr_b16 a[84:85], v112 offset:3072\n"
    "v_lshlrev_b32_sdwa v67, 1, v67 dst_sel:DWORD dst_unused:UNUSED_PAD src0_sel:DWORD src1_sel:WORD_1\n"
    "ds_read_u16 v46, v46 offset:65472\n"
    "ds_read_b64_tr_b16 a[86:87], v112 offset:3584\n"
    "v_lshlrev_b32_sdwa v66, 1, v66 dst_sel:DWORD dst_unused:UNUSED_PAD src0_sel:DWORD src1_sel:WORD_1\n"
    "ds_read_b64_tr_b16 a[90:91], v113 offset:2048\n"
    "v_lshlrev_b32_sdwa v65, 1, v65 dst_sel:DWORD dst_unused:UNUSED_PAD src0_sel:DWORD src1_sel:WORD_1\n"
    "v_mfma_f32_32x32x16_bf16 v[16:31], a[76:79], a[52:55], v[16:31]\n"
    "ds_read_b64_tr_b16 a[94:95], v113 offset:2080\n"
    "v_lshlrev_b32_sdwa v64, 1, v64 dst_sel:DWORD dst_unused:UNUSED_PAD src0_sel:DWORD src1_sel:WORD_1\n"
    "ds_read_b64_tr_b16 a[126:127], v113 offset:3072\n"
    "ds_read_b64_tr_b16 v[6:7], v113 offset:3104\n"
    "v_rcp_f32_e32 v80, v80\n"
    "ds_read_u16_d16_hi v47, v47 offset:65472\n"
    "v_rcp_f32_e32 v81, v81\n"
    "v_mfma_f32_32x32x16_bf16 v[48:63], a[76:79], a[60:63], v[48:63]\n"
    "v_rcp_f32_e32 v82, v82\n"
    "ds_read_u16_d16_hi v79, v79 offset:65472\n"
    "v_rcp_f32_e32 v83, v83\n"
    "v_rcp_f32_e32 v84, v84\n"
    "v_mfma_f32_16x16x32_bf16 v[96:99], a[72:75], a[64:67], v[96:99]\n"
    "v_rcp_f32_e64 v85, v85\n"
    "ds_read_u16 v78, v78 offset:65472\n"
    "v_mfma_f32_16x16x32_bf16 v[96:99], a[76:79], a[68:71], v[96:99]\n"
    "v_rcp_f32_e32 v86, v86\n"
    "v_rcp_f32_e32 v87, v87\n"
    "ds_read_u16_d16_hi v77, v77 offset:65472\n"
    "v_rcp_f32_e32 v88, v88\n"
    "v_rcp_f32_e32 v89, v89\n"
    "v_rcp_f32_e64 v90, v90\n"
    "ds_read_u16 v76, v76 offset:65472\n"
    "v_rcp_f32_e32 v91, v91\n"
    "v_rcp_f32_e32 v92, v92\n"
    "v_rcp_f32_e32 v93, v93\n"
    "ds_read_u16_d16_hi v75, v75 offset:65472\n"
    "s_cmp_lt_u32 s44, 4\n"
    "s_cbranch_scc0 Lq2%=\n"
    "s_setprio 0\n"
    "Lq2%=:\n"
    "v_pk_mul_f32 v[32:33], v[32:33], v[80:81]\n"
    "v_pk_mul_f32 v[34:35], v[34:35], v[82:83]\n"
    "v_pk_mul_f32 v[36:37], v[36:37], v[84:85]\n"
    "v_pk_mul_f32 v[38:39], v[38:39], v[86:87]\n"
    "s_add_u32 m0, s47, 2048\n"
    "v_pk_mul_f32 v[40:41], v[40:41], v[88:89]\n"
    "ds_read_u16 v74, v74 offset:65472\n"
    "v_pk_mul_f32 v[42:43], v[42:43], v[90:91]\n"
    "s_waitcnt lgkmcnt(7)\n"
    "buffer_load_dwordx4 v116, s[24:27], s48 offen lds\n"
    "v_pk_mul_f32 v[44:45], v[44:45], v[92:93]\n"
    "buffer_load_dwordx4 v117, s[24:27], s48 offen offset:1024 lds\n"
    "s_add_u32 s48, s48, 64\n"
    "v_pk_fma_f32 v[80:81], v[16:17], v[16:17], s[50:51] neg_lo:[0,1,0] neg_hi:[0,1,0]\n"
    "v_pk_fma_f32 v[82:83], v[18:19], v[18:19], s[50:51] neg_lo:[0,1,0] neg_hi:[0,1,0]\n"
    "v_pk_fma_f32 v[84:85], v[20:21], v[20:21], s[50:51] neg_lo:[0,1,0] neg_hi:[0,1,0]\n"
    "v_pk_fma_f32 v[86:87], v[22:23], v[22:23], s[50:51] neg_lo:[0,1,0] neg_hi:[0,1,0]\n"
    "ds_read_u16_d16_hi v73, v73 offset:65472\n"
    "v_pk_fma_f32 v[88:89], v[24:25], v[24:25], s[50:51] neg_lo:[0,1,0] neg_hi:[0,1,0]\n"
    "v_pk_fma_f32 v[90:91], v[26:27], v[26:27], s[50:51] neg_lo:[0,1,0] neg_hi:[0,1,0]\n"
    "v_pk_fma_f32 v[92:93], v[28:29], v[28:29], s[50:51] neg_lo:[0,1,0] neg_hi:[0,1,0]\n"
    "v_pk_mul_f32 v[80:81], v[80:81], v[16:17]\n"
    "v_pk_mul_f32 v[82:83], v[82:83], v[18:19]\n"
    "v_pk_mul_f32 v[84:85], v[84:85], v[20:21]\n"
    "ds_read_u16 v72, v72 offset:65472\n"
    "v_pk_mul_f32 v[86:87], v[86:87], v[22:23]\n"
    "v_pk_mul_f32 v[88:89], v[88:89], v[24:25]\n"
    "v_pk_mul_f32 v[90:91], v[90:91], v[26:27]\n"
    "v_pk_mul_f32 v[92:93], v[92:93], v[28:29]\n"
    "v_cvt_pk_bf16_f32 v32, v32, v33\n"
    "ds_read_u16_d16_hi v71, v71 offset:65472\n"
    "v_cvt_pk_bf16_f32 v33, v34, v35\n"
    "v_cvt_pk_bf16_f32 v34, v36, v37\n"
    "v_cvt_pk_bf16_f32 v35, v38, v39\n"
    "v_cvt_pk_bf16_f32 v36, v40, v41\n"
    "v_cvt_pk_bf16_f32 v37, v42, v43\n"
    "v_cvt_pk_bf16_f32 v38, v44, v45\n"
    "ds_read_u16 v70, v70 offset:65472\n"
    "s_waitcnt lgkmcnt(10)\n"
    "v_or_b32_e32 v39, v46, v47\n"
    "v_or_b32_e32 v110, v100, v101\n"
    "v_or_b32_e32 v111, v102, v103\n"
    "v_mfma_f32_32x32x16_bf16 a[0:15], v[32:35], a[80:83], a[0:15]\n"
    "v_exp_f32_e32 v80, v80\n"
    "ds_read_u16_d16_hi v69, v69 offset:65472\n"
    "v_exp_f32_e32 v81, v81\n"
    "v_mfma_f32_32x32x16_bf16 a[0:15], v[36:39], a[84:87], a[0:15]\n"
    "v_exp_f32_e32 v82, v82\n"
    "v_exp_f32_e32 v83, v83\n"
    "ds_read_u16 v68, v68 offset:65472\n"
    "v_mfma_f32_16x16x32_bf16 a[32:35], v[108:111], a[88:91], 0\n"
    "v_exp_f32_e32 v84, v84\n"
    "v_exp_f32_e32 v85, v85\n"
    "ds_read_u16_d16_hi v67, v67 offset:65472\n"
    "v_mfma_f32_16x16x32_bf16 a[36:39], v[108:111], a[92:95], 0\n"
    "s_waitcnt vmcnt(4)\n"
    "ds_read_b64_tr_b16 a[72:73], v112 offset:4096\n"
    "v_exp_f32_e32 v86, v86\n"
    "v_mfma_f32_16x16x32_bf16 a[40:43], v[108:111], a[124:127], 0\n"
    "ds_read_b64_tr_b16 a[74:75], v112 offset:4608\n"
    "v_exp_f32_e64 v87, v87\n"
    "ds_read_u16 v66, v66 offset:65472\n"
    "v_mfma_f32_16x16x32_bf16 a[44:47], v[108:111], v[4:7], 0\n"
    "ds_read_b64_tr_b16 a[76:77], v112 offset:5120\n"
    "v_exp_f32_e64 v88, v88\n"
    "v_mfma_f32_16x16x16bf16_1k v[100:103], a[96:97], a[120:121], 0\n"
    "ds_read_b64_tr_b16 a[78:79], v112 offset:5632\n"
    "v_exp_f32_e64 v89, v89\n"
    "ds_read_u16_d16_hi v65, v65 offset:65472\n"
    "v_mfma_f32_32x32x8bf16_1k v[32:47], a[96:97], a[112:113], 0\n"
    "v_exp_f32_e32 v90, v90\n"
    "v_exp_f32_e32 v91, v91\n"
    "ds_read_u16 v64, v64 offset:65472\n"
    "v_exp_f32_e64 v92, v92\n"
    "v_exp_f32_e32 v93, v93\n"
    "s_nop 2\n"
    "v_mfma_f32_16x16x4bf16_1k v[80:95], a[122:123], a[122:123], v[80:95]\n"
    "v_lshlrev_b32_sdwa v96, 1, v96 dst_sel:DWORD dst_unused:UNUSED_PAD src0_sel:DWORD src1_sel:WORD_1\n"
    "ds_read_u16 v96, v96 offset:65472\n"
    "s_waitcnt lgkmcnt(15)\n"
    "v_or_b32_e32 v79, v78, v79\n"
    "v_lshlrev_b32_sdwa v97, 1, v97 dst_sel:DWORD dst_unused:UNUSED_PAD src0_sel:DWORD src1_sel:WORD_1\n"
    "v_or_b32_e64 v78, v76, v77\n"
    "v_lshlrev_b32_sdwa v98, 1, v98 dst_sel:DWORD dst_unused:UNUSED_PAD src0_sel:DWORD src1_sel:WORD_1\n"
    "s_add_u32 m0, s47, 4096\n"
    "v_or_b32_e64 v77, v74, v75\n"
    "v_lshlrev_b32_sdwa v99, 1, v99 dst_sel:DWORD dst_unused:UNUSED_PAD src0_sel:DWORD src1_sel:WORD_1\n"
    "ds_read_u16_d16_hi v97, v97 offset:65472\n"
    "s_waitcnt lgkmcnt(14)\n"
    "v_or_b32_e32 v76, v72, v73\n"
    "v_lshlrev_b32_sdwa v30, 1, v30 dst_sel:DWORD dst_unused:UNUSED_PAD src0_sel:DWORD src1_sel:WORD_1\n"
    "s_waitcnt lgkmcnt(4)\n"
    "buffer_load_dwordx4 v120, s[28:31], s49 offen lds\n"
    "v_or_b32_e32 v75, v70, v71\n"
    "v_lshlrev_b32_sdwa v31, 1, v31 dst_sel:DWORD dst_unused:UNUSED_PAD src0_sel:DWORD src1_sel:WORD_1\n"
    "v_or_b32_e32 v74, v68, v69\n"
    "v_or_b32_e32 v73, v66, v67\n"
    "ds_read_u16 v98, v98 offset:65472\n"
    "buffer_load_dwordx4 v120, s[28:31], s49 offen offset:1024 lds\n"
    "s_waitcnt lgkmcnt(3)\n"
    "v_or_b32_e32 v72, v64, v65\n"
    "v_mfma_f32_32x32x16_bf16 a[16:31], v[76:79], a[84:87], a[16:31]\n"
    "s_add_u32 s49, s49, 2048\n"
    "v_lshlrev_b32_sdwa v63, 1, v63 dst_sel:DWORD dst_unused:UNUSED_PAD src0_sel:DWORD src1_sel:WORD_1\n"
    "v_lshlrev_b32_sdwa v62, 1, v62 dst_sel:DWORD dst_unused:UNUSED_PAD src0_sel:DWORD src1_sel:WORD_1\n"
    "v_lshlrev_b32_sdwa v61, 1, v61 dst_sel:DWORD dst_unused:UNUSED_PAD src0_sel:DWORD src1_sel:WORD_1\n"
    "v_mfma_f32_32x32x16_bf16 a[16:31], v[72:75], a[80:83], a[16:31]\n"
    "ds_read_u16_d16_hi v99, v99 offset:65472\n"
    "v_lshlrev_b32_sdwa v60, 1, v60 dst_sel:DWORD dst_unused:UNUSED_PAD src0_sel:DWORD src1_sel:WORD_1\n"
    "v_lshlrev_b32_sdwa v59, 1, v59 dst_sel:DWORD dst_unused:UNUSED_PAD src0_sel:DWORD src1_sel:WORD_1\n"
    "v_lshlrev_b32_sdwa v58, 1, v58 dst_sel:DWORD dst_unused:UNUSED_PAD src0_sel:DWORD src1_sel:WORD_1\n"
    "v_lshlrev_b32_sdwa v57, 1, v57 dst_sel:DWORD dst_unused:UNUSED_PAD src0_sel:DWORD src1_sel:WORD_1\n"
    "v_mfma_f32_32x32x16_bf16 v[64:79], a[72:75], a[56:59], v[32:47]\n"
    "ds_read_u16 v30, v30 offset:65472\n"
    "v_lshlrev_b32_sdwa v56, 1, v56 dst_sel:DWORD dst_unused:UNUSED_PAD src0_sel:DWORD src1_sel:WORD_1\n"
    "v_lshlrev_b32_sdwa v55, 1, v55 dst_sel:DWORD dst_unused:UNUSED_PAD src0_sel:DWORD src1_sel:WORD_1\n"
    "v_lshlrev_b32_sdwa v54, 1, v54 dst_sel:DWORD dst_unused:UNUSED_PAD src0_sel:DWORD src1_sel:WORD_1\n"
    "s_waitcnt vmcnt(4)\n"
    "ds_read_b64_tr_b16 a[80:81], v112\n"
    "v_lshlrev_b32_sdwa v53, 1, v53 dst_sel:DWORD dst_unused:UNUSED_PAD src0_sel:DWORD src1_sel:WORD_1\n"
    "v_mfma_f32_32x32x16_bf16 v[32:47], a[72:75], a[48:51], v[32:47]\n"
    "ds_read_b64_tr_b16 a[82:83], v112 offset:512\n"
    "v_lshlrev_b32_sdwa v52, 1, v52 dst_sel:DWORD dst_unused:UNUSED_PAD src0_sel:DWORD src1_sel:WORD_1\n"
    "ds_read_u16_d16_hi v31, v31 offset:65472\n"
    "ds_read_b64_tr_b16 a[84:85], v112 offset:1024\n"
    "v_lshlrev_b32_sdwa v51, 1, v51 dst_sel:DWORD dst_unused:UNUSED_PAD src0_sel:DWORD src1_sel:WORD_1\n"
    "ds_read_b64_tr_b16 a[86:87], v112 offset:1536\n"
    "v_lshlrev_b32_sdwa v50, 1, v50 dst_sel:DWORD dst_unused:UNUSED_PAD src0_sel:DWORD src1_sel:WORD_1\n"
    "ds_read_b64_tr_b16 a[88:89], v113\n"
    "v_lshlrev_b32_sdwa v49, 1, v49 dst_sel:DWORD dst_unused:UNUSED_PAD src0_sel:DWORD src1_sel:WORD_1\n"
    "v_mfma_f32_32x32x16_bf16 v[32:47], a[76:79], a[52:55], v[32:47]\n"
    "ds_read_b64_tr_b16 a[92:93], v113 offset:32\n"
    "v_lshlrev_b32_sdwa v48, 1, v48 dst_sel:DWORD dst_unused:UNUSED_PAD src0_sel:DWORD src1_sel:WORD_1\n"
    "ds_read_b64_tr_b16 a[124:125], v113 offset:1024\n"
    "ds_read_b64_tr_b16 v[4:5], v113 offset:1056\n"
    "v_rcp_f32_e32 v80, v80\n"
    "ds_read_u16_d16_hi v63, v63 offset:65472\n"
    "v_rcp_f32_e32 v81, v81\n"
    "v_mfma_f32_32x32x16_bf16 v[64:79], a[76:79], a[60:63], v[64:79]\n"
    "v_rcp_f32_e32 v82, v82\n"
    "ds_read_u16 v62, v62 offset:65472\n"
    "v_rcp_f32_e32 v83, v83\n"
    "v_rcp_f32_e32 v84, v84\n"
    "v_mfma_f32_16x16x32_bf16 v[100:103], a[72:75], a[64:67], v[100:103]\n"
    "ds_read_u16_d16_hi v61, v61 offset:65472\n"
    "v_rcp_f32_e64 v85, v85\n"
    "v_mfma_f32_16x16x32_bf16 v[100:103], a[76:79], a[68:71], v[100:103]\n"
    "v_rcp_f32_e32 v86, v86\n"
    "v_rcp_f32_e32 v87, v87\n"
    "ds_read_u16 v60, v60 offset:65472\n"
    "v_rcp_f32_e32 v88, v88\n"
    "v_rcp_f32_e32 v89, v89\n"
    "v_rcp_f32_e64 v90, v90\n"
    "ds_read_u16_d16_hi v59, v59 offset:65472\n"
    "v_rcp_f32_e32 v91, v91\n"
    "v_rcp_f32_e32 v92, v92\n"
    "v_rcp_f32_e32 v93, v93\n"
    "ds_read_u16 v58, v58 offset:65472\n"
    "v_pk_mul_f32 v[16:17], v[16:17], v[80:81]\n"
    "v_pk_mul_f32 v[18:19], v[18:19], v[82:83]\n"
    "v_pk_mul_f32 v[20:21], v[20:21], v[84:85]\n"
    "v_pk_mul_f32 v[22:23], v[22:23], v[86:87]\n"
    "ds_read_u16_d16_hi v57, v57 offset:65472\n"
    "s_mov_b32 m0, s47\n"
    "v_pk_mul_f32 v[24:25], v[24:25], v[88:89]\n"
    "v_pk_mul_f32 v[26:27], v[26:27], v[90:91]\n"
    "s_waitcnt lgkmcnt(7)\n"
    "buffer_load_dwordx4 v116, s[24:27], s48 offen lds\n"
    "v_pk_mul_f32 v[28:29], v[28:29], v[92:93]\n"
    "buffer_load_dwordx4 v117, s[24:27], s48 offen offset:1024 lds\n"
    "s_add_u32 s48, s48, 64\n"
    "v_pk_fma_f32 v[80:81], v[32:33], v[32:33], s[50:51] neg_lo:[0,1,0] neg_hi:[0,1,0]\n"
    "v_pk_fma_f32 v[82:83], v[34:35], v[34:35], s[50:51] neg_lo:[0,1,0] neg_hi:[0,1,0]\n"
    "v_pk_fma_f32 v[84:85], v[36:37], v[36:37], s[50:51] neg_lo:[0,1,0] neg_hi:[0,1,0]\n"
    "ds_read_u16 v56, v56 offset:65472\n"
    "v_pk_fma_f32 v[86:87], v[38:39], v[38:39], s[50:51] neg_lo:[0,1,0] neg_hi:[0,1,0]\n"
    "v_pk_fma_f32 v[88:89], v[40:41], v[40:41], s[50:51] neg_lo:[0,1,0] neg_hi:[0,1,0]\n"
    "v_pk_fma_f32 v[90:91], v[42:43], v[42:43], s[50:51] neg_lo:[0,1,0] neg_hi:[0,1,0]\n"
    "v_pk_fma_f32 v[92:93], v[44:45], v[44:45], s[50:51] neg_lo:[0,1,0] neg_hi:[0,1,0]\n"
    "v_pk_mul_f32 v[80:81], v[80:81], v[32:33]\n"
    "v_pk_mul_f32 v[82:83], v[82:83], v[34:35]\n"
    "ds_read_u16_d16_hi v55, v55 offset:65472\n"
    "v_pk_mul_f32 v[84:85], v[84:85], v[36:37]\n"
    "v_pk_mul_f32 v[86:87], v[86:87], v[38:39]\n"
    "v_pk_mul_f32 v[88:89], v[88:89], v[40:41]\n"
    "v_pk_mul_f32 v[90:91], v[90:91], v[42:43]\n"
    "v_pk_mul_f32 v[92:93], v[92:93], v[44:45]\n"
    "v_cvt_pk_bf16_f32 v16, v16, v17\n"
    "ds_read_u16 v54, v54 offset:65472\n"
    "v_cvt_pk_bf16_f32 v17, v18, v19\n"
    "v_cvt_pk_bf16_f32 v18, v20, v21\n"
    "v_cvt_pk_bf16_f32 v19, v22, v23\n"
    "v_cvt_pk_bf16_f32 v20, v24, v25\n"
    "v_cvt_pk_bf16_f32 v21, v26, v27\n"
    "ds_read_u16_d16_hi v53, v53 offset:65472\n"
    "v_cvt_pk_bf16_f32 v22, v28, v29\n"
    "v_or_b32_e32 v23, v30, v31\n"
    "v_or_b32_e32 v108, v96, v97\n"
    "v_or_b32_e32 v109, v98, v99\n"
    "v_mfma_f32_32x32x16_bf16 a[0:15], v[16:19], a[80:83], a[0:15]\n"
    "v_exp_f32_e32 v80, v80\n"
    "ds_read_u16 v52, v52 offset:65472\n"
    "v_exp_f32_e32 v81, v81\n"
    "v_mfma_f32_32x32x16_bf16 a[0:15], v[20:23], a[84:87], a[0:15]\n"
    "v_exp_f32_e32 v82, v82\n"
    "ds_read_u16_d16_hi v51, v51 offset:65472\n"
    "v_exp_f32_e32 v83, v83\n"
    "v_exp_f32_e32 v84, v84\n"
    "v_exp_f32_e32 v85, v85\n"
    "ds_read_u16 v50, v50 offset:65472\n"
    "s_waitcnt vmcnt(4)\n"
    "ds_read_b64_tr_b16 a[72:73], v112 offset:2048\n"
    "v_exp_f32_e32 v86, v86\n"
    "ds_read_b64_tr_b16 a[74:75], v112 offset:2560\n"
    "v_exp_f32_e32 v87, v87\n"
    "ds_read_b64_tr_b16 a[76:77], v112 offset:3072\n"
    "v_exp_f32_e64 v88, v88\n"
    "ds_read_u16_d16_hi v49, v49 offset:65472\n"
    "v_mfma_f32_16x16x16bf16_1k v[96:99], a[98:99], a[114:115], 0\n"
    "ds_read_b64_tr_b16 a[78:79], v112 offset:3584\n"
    "v_exp_f32_e32 v89, v89\n"
    "v_mfma_f32_32x32x8bf16_1k v[16:31], a[98:99], a[106:107], 0\n"
    "v_exp_f32_e32 v90, v90\n"
    "ds_read_u16 v48, v48 offset:65472\n"
    "v_exp_f32_e32 v91, v91\n"
    "v_exp_f32_e32 v92, v92\n"
    "v_exp_f32_e32 v93, v93\n"
    "s_nop 2\n"
    "v_mfma_f32_16x16x4bf16_1k v[80:95], a[122:123], a[122:123], v[80:95]\n"
    "v_lshlrev_b32_sdwa v100, 1, v100 dst_sel:DWORD dst_unused:UNUSED_PAD src0_sel:DWORD src1_sel:WORD_1\n"
    "ds_read_u16 v100, v100 offset:65472\n"
    "s_waitcnt lgkmcnt(15)\n"
    "v_or_b32_e32 v63, v62, v63\n"
    "v_lshlrev_b32_sdwa v101, 1, v101 dst_sel:DWORD dst_unused:UNUSED_PAD src0_sel:DWORD src1_sel:WORD_1\n"
    "v_or_b32_e64 v62, v60, v61\n"
    "v_lshlrev_b32_sdwa v102, 1, v102 dst_sel:DWORD dst_unused:UNUSED_PAD src0_sel:DWORD src1_sel:WORD_1\n"
    "s_add_u32 m0, s47, 2048\n"
    "v_or_b32_e64 v61, v58, v59\n"
    "v_lshlrev_b32_sdwa v103, 1, v103 dst_sel:DWORD dst_unused:UNUSED_PAD src0_sel:DWORD src1_sel:WORD_1\n"
    "ds_read_u16_d16_hi v101, v101 offset:65472\n"
    "s_waitcnt lgkmcnt(14)\n"
    "v_or_b32_e32 v60, v56, v57\n"
    "v_lshlrev_b32_sdwa v46, 1, v46 dst_sel:DWORD dst_unused:UNUSED_PAD src0_sel:DWORD src1_sel:WORD_1\n"
    "s_waitcnt lgkmcnt(3)\n"
    "buffer_load_dwordx4 v120, s[28:31], s49 offen lds\n"
    "v_or_b32_e32 v59, v54, v55\n"
    "v_lshlrev_b32_sdwa v47, 1, v47 dst_sel:DWORD dst_unused:UNUSED_PAD src0_sel:DWORD src1_sel:WORD_1\n"
    "v_or_b32_e32 v58, v52, v53\n"
    "v_or_b32_e32 v57, v50, v51\n"
    "ds_read_u16 v102, v102 offset:65472\n"
    "buffer_load_dwordx4 v120, s[28:31], s49 offen offset:1024 lds\n"
    "s_waitcnt lgkmcnt(3)\n"
    "v_or_b32_e32 v56, v48, v49\n"
    "v_mfma_f32_32x32x16_bf16 a[16:31], v[60:63], a[84:87], a[16:31]\n"
    "s_add_u32 s49, s49, 2048\n"
    "v_lshlrev_b32_sdwa v79, 1, v79 dst_sel:DWORD dst_unused:UNUSED_PAD src0_sel:DWORD src1_sel:WORD_1\n"
    "v_lshlrev_b32_sdwa v78, 1, v78 dst_sel:DWORD dst_unused:UNUSED_PAD src0_sel:DWORD src1_sel:WORD_1\n"
    "v_lshlrev_b32_sdwa v77, 1, v77 dst_sel:DWORD dst_unused:UNUSED_PAD src0_sel:DWORD src1_sel:WORD_1\n"
    "v_mfma_f32_32x32x16_bf16 a[16:31], v[56:59], a[80:83], a[16:31]\n"
    "ds_read_u16_d16_hi v103, v103 offset:65472\n"
    "v_lshlrev_b32_sdwa v76, 1, v76 dst_sel:DWORD dst_unused:UNUSED_PAD src0_sel:DWORD src1_sel:WORD_1\n"
    "v_lshlrev_b32_sdwa v75, 1, v75 dst_sel:DWORD dst_unused:UNUSED_PAD src0_sel:DWORD src1_sel:WORD_1\n"
    "v_lshlrev_b32_sdwa v74, 1, v74 dst_sel:DWORD dst_unused:UNUSED_PAD src0_sel:DWORD src1_sel:WORD_1\n"
    "v_lshlrev_b32_sdwa v73, 1, v73 dst_sel:DWORD dst_unused:UNUSED_PAD src0_sel:DWORD src1_sel:WORD_1\n"
    "v_mfma_f32_32x32x16_bf16 v[48:63], a[72:75], a[56:59], v[16:31]\n"
    "ds_read_u16 v46, v46 offset:65472\n"
    "v_lshlrev_b32_sdwa v72, 1, v72 dst_sel:DWORD dst_unused:UNUSED_PAD src0_sel:DWORD src1_sel:WORD_1\n"
    "v_lshlrev_b32_sdwa v71, 1, v71 dst_sel:DWORD dst_unused:UNUSED_PAD src0_sel:DWORD src1_sel:WORD_1\n"
    "v_lshlrev_b32_sdwa v70, 1, v70 dst_sel:DWORD dst_unused:UNUSED_PAD src0_sel:DWORD src1_sel:WORD_1\n"
    "s_waitcnt vmcnt(4)\n"
    "ds_read_b64_tr_b16 a[80:81], v112 offset:4096\n"
    "v_lshlrev_b32_sdwa v69, 1, v69 dst_sel:DWORD dst_unused:UNUSED_PAD src0_sel:DWORD src1_sel:WORD_1\n"
    "v_mfma_f32_32x32x16_bf16 v[16:31], a[72:75], a[48:51], v[16:31]\n"
    "ds_read_b64_tr_b16 a[82:83], v112 offset:4608\n"
    "v_lshlrev_b32_sdwa v68, 1, v68 dst_sel:DWORD dst_unused:UNUSED_PAD src0_sel:DWORD src1_sel:WORD_1\n"
    "ds_read_u16_d16_hi v47, v47 offset:65472\n"
    "ds_read_b64_tr_b16 a[84:85], v112 offset:5120\n"
    "v_lshlrev_b32_sdwa v67, 1, v67 dst_sel:DWORD dst_unused:UNUSED_PAD src0_sel:DWORD src1_sel:WORD_1\n"
    "ds_read_b64_tr_b16 a[86:87], v112 offset:5632\n"
    "v_lshlrev_b32_sdwa v66, 1, v66 dst_sel:DWORD dst_unused:UNUSED_PAD src0_sel:DWORD src1_sel:WORD_1\n"
    "ds_read_b64_tr_b16 a[90:91], v113 offset:4096\n"
    "v_lshlrev_b32_sdwa v65, 1, v65 dst_sel:DWORD dst_unused:UNUSED_PAD src0_sel:DWORD src1_sel:WORD_1\n"
    "v_mfma_f32_32x32x16_bf16 v[16:31], a[76:79], a[52:55], v[16:31]\n"
    "ds_read_b64_tr_b16 a[94:95], v113 offset:4128\n"
    "v_lshlrev_b32_sdwa v64, 1, v64 dst_sel:DWORD dst_unused:UNUSED_PAD src0_sel:DWORD src1_sel:WORD_1\n"
    "ds_read_b64_tr_b16 a[126:127], v113 offset:5120\n"
    "ds_read_b64_tr_b16 v[6:7], v113 offset:5152\n"
    "v_rcp_f32_e32 v80, v80\n"
    "ds_read_u16_d16_hi v79, v79 offset:65472\n"
    "v_rcp_f32_e32 v81, v81\n"
    "v_mfma_f32_32x32x16_bf16 v[48:63], a[76:79], a[60:63], v[48:63]\n"
    "v_rcp_f32_e32 v82, v82\n"
    "ds_read_u16 v78, v78 offset:65472\n"
    "v_rcp_f32_e32 v83, v83\n"
    "v_rcp_f32_e32 v84, v84\n"
    "v_mfma_f32_16x16x32_bf16 v[96:99], a[72:75], a[64:67], v[96:99]\n"
    "ds_read_u16_d16_hi v77, v77 offset:65472\n"
    "v_rcp_f32_e64 v85, v85\n"
    "v_mfma_f32_16x16x32_bf16 v[96:99], a[76:79], a[68:71], v[96:99]\n"
    "v_rcp_f32_e32 v86, v86\n"
    "v_rcp_f32_e32 v87, v87\n"
    "ds_read_u16 v76, v76 offset:65472\n"
    "v_rcp_f32_e32 v88, v88\n"
    "v_rcp_f32_e32 v89, v89\n"
    "v_rcp_f32_e64 v90, v90\n"
    "ds_read_u16_d16_hi v75, v75 offset:65472\n"
    "v_rcp_f32_e32 v91, v91\n"
    "v_rcp_f32_e32 v92, v92\n"
    "v_rcp_f32_e32 v93, v93\n"
    "ds_read_u16 v74, v74 offset:65472\n"
    "s_cmp_lt_u32 s44, 4\n"
    "s_cbranch_scc0 Lq4%=\n"
    "s_setprio 2\n"
    "Lq4%=:\n"
    "v_pk_mul_f32 v[32:33], v[32:33], v[80:81]\n"
    "v_pk_mul_f32 v[34:35], v[34:35], v[82:83]\n"
    "v_pk_mul_f32 v[36:37], v[36:37], v[84:85]\n"
    "v_pk_mul_f32 v[38:39], v[38:39], v[86:87]\n"
    "ds_read_u16_d16_hi v73, v73 offset:65472\n"
    "s_add_u32 m0, s47, 4096\n"
    "v_pk_mul_f32 v[40:41], v[40:41], v[88:89]\n"
    "v_pk_mul_f32 v[42:43], v[42:43], v[90:91]\n"
    "s_waitcnt lgkmcnt(7)\n"
    "buffer_load_dwordx4 v116, s[24:27], s48 offen lds\n"
    "v_pk_mul_f32 v[44:45], v[44:45], v[92:93]\n"
    "buffer_load_dwordx4 v117, s[24:27], s48 offen offset:1024 lds\n"
    "s_add_u32 s48, s48, 64\n"
    "v_pk_fma_f32 v[80:81], v[16:17], v[16:17], s[50:51] neg_lo:[0,1,0] neg_hi:[0,1,0]\n"
    "v_pk_fma_f32 v[82:83], v[18:19], v[18:19], s[50:51] neg_lo:[0,1,0] neg_hi:[0,1,0]\n"
    "v_pk_fma_f32 v[84:85], v[20:21], v[20:21], s[50:51] neg_lo:[0,1,0] neg_hi:[0,1,0]\n"
    "ds_read_u16 v72, v72 offset:65472\n"
    "v_pk_fma_f32 v[86:87], v[22:23], v[22:23], s[50:51] neg_lo:[0,1,0] neg_hi:[0,1,0]\n"
    "v_pk_fma_f32 v[88:89], v[24:25], v[24:25], s[50:51] neg_lo:[0,1,0] neg_hi:[0,1,0]\n"
    "v_pk_fma_f32 v[90:91], v[26:27], v[26:27], s[50:51] neg_lo:[0,1,0] neg_hi:[0,1,0]\n"
    "v_pk_fma_f32 v[92:93], v[28:29], v[28:29], s[50:51] neg_lo:[0,1,0] neg_hi:[0,1,0]\n"
    "v_pk_mul_f32 v[80:81], v[80:81], v[16:17]\n"
    "v_pk_mul_f32 v[82:83], v[82:83], v[18:19]\n"
    "ds_read_u16_d16_hi v71, v71 offset:65472\n"
    "v_pk_mul_f32 v[84:85], v[84:85], v[20:21]\n"
    "v_pk_mul_f32 v[86:87], v[86:87], v[22:23]\n"
    "v_pk_mul_f32 v[88:89], v[88:89], v[24:25]\n"
    "v_pk_mul_f32 v[90:91], v[90:91], v[26:27]\n"
    "v_pk_mul_f32 v[92:93], v[92:93], v[28:29]\n"
    "v_cvt_pk_bf16_f32 v32, v32, v33\n"
    "ds_read_u16 v70, v70 offset:65472\n"
    "v_cvt_pk_bf16_f32 v33, v34, v35\n"
    "v_cvt_pk_bf16_f32 v34, v36, v37\n"
    "v_cvt_pk_bf16_f32 v35, v38, v39\n"
    "v_cvt_pk_bf16_f32 v36, v40, v41\n"
    "v_cvt_pk_bf16_f32 v37, v42, v43\n"
    "ds_read_u16_d16_hi v69, v69 offset:65472\n"
    "v_cvt_pk_bf16_f32 v38, v44, v45\n"
    "v_or_b32_e32 v39, v46, v47\n"
    "v_or_b32_e32 v110, v100, v101\n"
    "v_or_b32_e32 v111, v102, v103\n"
    "v_mfma_f32_32x32x16_bf16 a[0:15], v[32:35], a[80:83], a[0:15]\n"
    "v_exp_f32_e32 v80, v80\n"
    "ds_read_u16 v68, v68 offset:65472\n"
    "v_exp_f32_e32 v81, v81\n"
    "v_mfma_f32_32x32x16_bf16 a[0:15], v[36:39], a[84:87], a[0:15]\n"
    "v_exp_f32_e32 v82, v82\n"
    "ds_read_u16_d16_hi v67, v67 offset:65472\n"
    "v_exp_f32_e64 v83, v83\n"
    "v_mfma_f32_16x16x32_bf16 a[32:35], v[108:111], a[88:91], a[32:35]\n"
    "v_exp_f32_e32 v84, v84\n"
    "v_exp_f32_e32 v85, v85\n"
    "ds_read_u16 v66, v66 offset:65472\n"
    "v_mfma_f32_16x16x32_bf16 a[36:39], v[108:111], a[92:95], a[36:39]\n"
    "s_waitcnt vmcnt(4)\n"
    "ds_read_b64_tr_b16 a[72:73], v112\n"
    "v_exp_f32_e32 v86, v86\n"
    "v_mfma_f32_16x16x32_bf16 a[40:43], v[108:111], a[124:127], a[40:43]\n"
    "ds_read_u16_d16_hi v65, v65 offset:65472\n"
    "ds_read_b64_tr_b16 a[74:75], v112 offset:512\n"
    "v_exp_f32_e64 v87, v87\n"
    "v_mfma_f32_16x16x32_bf16 a[44:47], v[108:111], v[4:7], a[44:47]\n"
    "ds_read_b64_tr_b16 a[76:77], v112 offset:1024\n"
    "v_exp_f32_e64 v88, v88\n"
    "v_mfma_f32_16x16x16bf16_1k v[100:103], a[98:99], a[116:117], 0\n"
    "ds_read_u16 v64, v64 offset:65472\n"
    "ds_read_b64_tr_b16 a[78:79], v112 offset:1536\n"
    "v_exp_f32_e32 v89, v89\n"
    "v_mfma_f32_32x32x8bf16_1k v[32:47], a[98:99], a[108:109], 0\n"
    "v_exp_f32_e32 v90, v90\n"
    "v_exp_f32_e32 v91, v91\n"
    "v_exp_f32_e32 v92, v92\n"
    "v_exp_f32_e32 v93, v93\n"
    "s_nop 2\n"
    "v_mfma_f32_16x16x4bf16_1k v[80:95], a[122:123], a[122:123], v[80:95]\n"
    "v_lshlrev_b32_sdwa v96, 1, v96 dst_sel:DWORD dst_unused:UNUSED_PAD src0_sel:DWORD src1_sel:WORD_1\n"
    "ds_read_u16 v96, v96 offset:65472\n"
    "s_waitcnt lgkmcnt(15)\n"
    "v_or_b32_e32 v79, v78, v79\n"
    "v_lshlrev_b32_sdwa v97, 1, v97 dst_sel:DWORD dst_unused:UNUSED_PAD src0_sel:DWORD src1_sel:WORD_1\n"
    "v_or_b32_e64 v78, v76, v77\n"
    "v_lshlrev_b32_sdwa v98, 1, v98 dst_sel:DWORD dst_unused:UNUSED_PAD src0_sel:DWORD src1_sel:WORD_1\n"
    "s_mov_b32 m0, s47\n"
    "v_or_b32_e32 v77, v74, v75\n"
    "v_lshlrev_b32_sdwa v99, 1, v99 dst_sel:DWORD dst_unused:UNUSED_PAD src0_sel:DWORD src1_sel:WORD_1\n"
    "ds_read_u16_d16_hi v97, v97 offset:65472\n"
    "s_waitcnt lgkmcnt(14)\n"
    "v_or_b32_e32 v76, v72, v73\n"
    "v_lshlrev_b32_sdwa v30, 1, v30 dst_sel:DWORD dst_unused:UNUSED_PAD src0_sel:DWORD src1_sel:WORD_1\n"
    "s_waitcnt lgkmcnt(2)\n"
    "buffer_load_dwordx4 v120, s[28:31], s49 offen lds\n"
    "v_or_b32_e32 v75, v70, v71\n"
    "v_lshlrev_b32_sdwa v31, 1, v31 dst_sel:DWORD dst_unused:UNUSED_PAD src0_sel:DWORD src1_sel:WORD_1\n"
    "v_or_b32_e32 v74, v68, v69\n"
    "v_or_b32_e32 v73, v66, v67\n"
    "ds_read_u16 v98, v98 offset:65472\n"
    "buffer_load_dwordx4 v120, s[28:31], s49 offen offset:1024 lds\n"
    "v_or_b32_e32 v72, v64, v65\n"
    "v_mfma_f32_32x32x16_bf16 a[16:31], v[76:79], a[84:87], a[16:31]\n"
    "s_add_u32 s49, s49, 2048\n"
    "v_lshlrev_b32_sdwa v63, 1, v63 dst_sel:DWORD dst_unused:UNUSED_PAD src0_sel:DWORD src1_sel:WORD_1\n"
    "v_lshlrev_b32_sdwa v62, 1, v62 dst_sel:DWORD dst_unused:UNUSED_PAD src0_sel:DWORD src1_sel:WORD_1\n"
    "v_lshlrev_b32_sdwa v61, 1, v61 dst_sel:DWORD dst_unused:UNUSED_PAD src0_sel:DWORD src1_sel:WORD_1\n"
    "v_mfma_f32_32x32x16_bf16 a[16:31], v[72:75], a[80:83], a[16:31]\n"
    "ds_read_u16_d16_hi v99, v99 offset:65472\n"
    "v_lshlrev_b32_sdwa v60, 1, v60 dst_sel:DWORD dst_unused:UNUSED_PAD src0_sel:DWORD src1_sel:WORD_1\n"
    "v_lshlrev_b32_sdwa v59, 1, v59 dst_sel:DWORD dst_unused:UNUSED_PAD src0_sel:DWORD src1_sel:WORD_1\n"
    "v_lshlrev_b32_sdwa v58, 1, v58 dst_sel:DWORD dst_unused:UNUSED_PAD src0_sel:DWORD src1_sel:WORD_1\n"
    "v_lshlrev_b32_sdwa v57, 1, v57 dst_sel:DWORD dst_unused:UNUSED_PAD src0_sel:DWORD src1_sel:WORD_1\n"
    "v_mfma_f32_32x32x16_bf16 v[64:79], a[72:75], a[56:59], v[32:47]\n"
    "ds_read_u16 v30, v30 offset:65472\n"
    "v_lshlrev_b32_sdwa v56, 1, v56 dst_sel:DWORD dst_unused:UNUSED_PAD src0_sel:DWORD src1_sel:WORD_1\n"
    "v_lshlrev_b32_sdwa v55, 1, v55 dst_sel:DWORD dst_unused:UNUSED_PAD src0_sel:DWORD src1_sel:WORD_1\n"
    "v_lshlrev_b32_sdwa v54, 1, v54 dst_sel:DWORD dst_unused:UNUSED_PAD src0_sel:DWORD src1_sel:WORD_1\n"
    "s_waitcnt vmcnt(4)\n"
    "ds_read_b64_tr_b16 a[80:81], v112 offset:2048\n"
    "v_lshlrev_b32_sdwa v53, 1, v53 dst_sel:DWORD dst_unused:UNUSED_PAD src0_sel:DWORD src1_sel:WORD_1\n"
    "v_mfma_f32_32x32x16_bf16 v[32:47], a[72:75], a[48:51], v[32:47]\n"
    "ds_read_b64_tr_b16 a[82:83], v112 offset:2560\n"
    "v_lshlrev_b32_sdwa v52, 1, v52 dst_sel:DWORD dst_unused:UNUSED_PAD src0_sel:DWORD src1_sel:WORD_1\n"
    "ds_read_u16_d16_hi v31, v31 offset:65472\n"
    "ds_read_b64_tr_b16 a[84:85], v112 offset:3072\n"
    "v_lshlrev_b32_sdwa v51, 1, v51 dst_sel:DWORD dst_unused:UNUSED_PAD src0_sel:DWORD src1_sel:WORD_1\n"
    "ds_read_b64_tr_b16 a[86:87], v112 offset:3584\n"
    "v_lshlrev_b32_sdwa v50, 1, v50 dst_sel:DWORD dst_unused:UNUSED_PAD src0_sel:DWORD src1_sel:WORD_1\n"
    "ds_read_b64_tr_b16 a[88:89], v113 offset:2048\n"
    "v_lshlrev_b32_sdwa v49, 1, v49 dst_sel:DWORD dst_unused:UNUSED_PAD src0_sel:DWORD src1_sel:WORD_1\n"
    "v_mfma_f32_32x32x16_bf16 v[32:47], a[76:79], a[52:55], v[32:47]\n"
    "ds_read_b64_tr_b16 a[92:93], v113 offset:2080\n"
    "v_lshlrev_b32_sdwa v48, 1, v48 dst_sel:DWORD dst_unused:UNUSED_PAD src0_sel:DWORD src1_sel:WORD_1\n"
    "ds_read_b64_tr_b16 a[124:125], v113 offset:3072\n"
    "ds_read_b64_tr_b16 v[4:5], v113 offset:3104\n"
    "v_rcp_f32_e32 v80, v80\n"
    "ds_read_u16_d16_hi v63, v63 offset:65472\n"
    "v_rcp_f32_e32 v81, v81\n"
    "v_mfma_f32_32x32x16_bf16 v[64:79], a[76:79], a[60:63], v[64:79]\n"
    "v_rcp_f32_e64 v82, v82\n"
    "ds_read_u16 v62, v62 offset:65472\n"
    "v_rcp_f32_e32 v83, v83\n"
    "v_rcp_f32_e32 v84, v84\n"
    "v_mfma_f32_16x16x32_bf16 v[100:103], a[72:75], a[64:67], v[100:103]\n"
    "ds_read_u16_d16_hi v61, v61 offset:65472\n"
    "v_rcp_f32_e64 v85, v85\n"
    "v_mfma_f32_16x16x32_bf16 v[100:103], a[76:79], a[68:71], v[100:103]\n"
    "v_rcp_f32_e32 v86, v86\n"
    "v_rcp_f32_e32 v87, v87\n"
    "ds_read_u16 v60, v60 offset:65472\n"
    "v_rcp_f32_e32 v88, v88\n"
    "v_rcp_f32_e32 v89, v89\n"
    "v_rcp_f32_e32 v90, v90\n"
    "ds_read_u16_d16_hi v59, v59 offset:65472\n"
    "v_rcp_f32_e32 v91, v91\n"
    "v_rcp_f32_e32 v92, v92\n"
    "v_rcp_f32_e32 v93, v93\n"
    "ds_read_u16 v58, v58 offset:65472\n"
    "v_pk_mul_f32 v[16:17], v[16:17], v[80:81]\n"
    "v_pk_mul_f32 v[18:19], v[18:19], v[82:83]\n"
    "v_pk_mul_f32 v[20:21], v[20:21], v[84:85]\n"
    "v_pk_mul_f32 v[22:23], v[22:23], v[86:87]\n"
    "ds_read_u16_d16_hi v57, v57 offset:65472\n"
    "s_add_u32 m0, s47, 2048\n"
    "v_pk_mul_f32 v[24:25], v[24:25], v[88:89]\n"
    "v_pk_mul_f32 v[26:27], v[26:27], v[90:91]\n"
    "s_waitcnt lgkmcnt(7)\n"
    "buffer_load_dwordx4 v116, s[24:27], s48 offen lds\n"
    "v_pk_mul_f32 v[28:29], v[28:29], v[92:93]\n"
    "buffer_load_dwordx4 v117, s[24:27], s48 offen offset:1024 lds\n"
    "s_add_u32 s48, s48, 64\n"
    "v_pk_fma_f32 v[80:81], v[32:33], v[32:33], s[50:51] neg_lo:[0,1,0] neg_hi:[0,1,0]\n"
    "v_pk_fma_f32 v[82:83], v[34:35], v[34:35], s[50:51] neg_lo:[0,1,0] neg_hi:[0,1,0]\n"
    "v_pk_fma_f32 v[84:85], v[36:37], v[36:37], s[50:51] neg_lo:[0,1,0] neg_hi:[0,1,0]\n"
    "ds_read_u16 v56, v56 offset:65472\n"
    "v_pk_fma_f32 v[86:87], v[38:39], v[38:39], s[50:51] neg_lo:[0,1,0] neg_hi:[0,1,0]\n"
    "v_pk_fma_f32 v[88:89], v[40:41], v[40:41], s[50:51] neg_lo:[0,1,0] neg_hi:[0,1,0]\n"
    "v_pk_fma_f32 v[90:91], v[42:43], v[42:43], s[50:51] neg_lo:[0,1,0] neg_hi:[0,1,0]\n"
    "v_pk_fma_f32 v[92:93], v[44:45], v[44:45], s[50:51] neg_lo:[0,1,0] neg_hi:[0,1,0]\n"
    "v_pk_mul_f32 v[80:81], v[80:81], v[32:33]\n"
    "v_pk_mul_f32 v[82:83], v[82:83], v[34:35]\n"
    "ds_read_u16_d16_hi v55, v55 offset:65472\n"
    "v_pk_mul_f32 v[84:85], v[84:85], v[36:37]\n"
    "v_pk_mul_f32 v[86:87], v[86:87], v[38:39]\n"
    "v_pk_mul_f32 v[88:89], v[88:89], v[40:41]\n"
    "v_pk_mul_f32 v[90:91], v[90:91], v[42:43]\n"
    "v_pk_mul_f32 v[92:93], v[92:93], v[44:45]\n"
    "v_cvt_pk_bf16_f32 v16, v16, v17\n"
    "ds_read_u16 v54, v54 offset:65472\n"
    "v_cvt_pk_bf16_f32 v17, v18, v19\n"
    "v_cvt_pk_bf16_f32 v18, v20, v21\n"
    "v_cvt_pk_bf16_f32 v19, v22, v23\n"
    "v_cvt_pk_bf16_f32 v20, v24, v25\n"
    "v_cvt_pk_bf16_f32 v21, v26, v27\n"
    "ds_read_u16_d16_hi v53, v53 offset:65472\n"
    "v_cvt_pk_bf16_f32 v22, v28, v29\n"
    "v_or_b32_e32 v23, v30, v31\n"
    "v_or_b32_e32 v108, v96, v97\n"
    "v_or_b32_e32 v109, v98, v99\n"
    "v_mfma_f32_32x32x16_bf16 a[0:15], v[16:19], a[80:83], a[0:15]\n"
    "v_exp_f32_e32 v80, v80\n"
    "ds_read_u16 v52, v52 offset:65472\n"
    "v_exp_f32_e32 v81, v81\n"
    "v_mfma_f32_32x32x16_bf16 a[0:15], v[20:23], a[84:87], a[0:15]\n"
    "v_exp_f32_e32 v82, v82\n"
    "ds_read_u16_d16_hi v51, v51 offset:65472\n"
    "v_exp_f32_e32 v83, v83\n"
    "v_exp_f32_e32 v84, v84\n"
    "v_exp_f32_e32 v85, v85\n"
    "ds_read_u16 v50, v50 offset:65472\n"
    "s_waitcnt vmcnt(4)\n"
    "ds_read_b64_tr_b16 a[72:73], v112 offset:4096\n"
    "v_exp_f32_e32 v86, v86\n"
    "ds_read_b64_tr_b16 a[74:75], v112 offset:4608\n"
    "v_exp_f32_e32 v87, v87\n"
    "ds_read_b64_tr_b16 a[76:77], v112 offset:5120\n"
    "v_exp_f32_e64 v88, v88\n"
    "ds_read_u16_d16_hi v49, v49 offset:65472\n"
    "v_mfma_f32_16x16x16bf16_1k v[96:99], a[98:99], a[118:119], 0\n"
    "ds_read_b64_tr_b16 a[78:79], v112 offset:5632\n"
    "v_exp_f32_e32 v89, v89\n"
    "v_mfma_f32_32x32x8bf16_1k v[16:31], a[98:99], a[110:111], 0\n"
    "v_exp_f32_e32 v90, v90\n"
    "ds_read_u16 v48, v48 offset:65472\n"
    "v_exp_f32_e32 v91, v91\n"
    "v_exp_f32_e32 v92, v92\n"
    "v_exp_f32_e32 v93, v93\n"
    "s_nop 2\n"
    "v_mfma_f32_16x16x4bf16_1k v[80:95], a[122:123], a[122:123], v[80:95]\n"
    "v_lshlrev_b32_sdwa v100, 1, v100 dst_sel:DWORD dst_unused:UNUSED_PAD src0_sel:DWORD src1_sel:WORD_1\n"
    "ds_read_u16 v100, v100 offset:65472\n"
    "s_waitcnt lgkmcnt(15)\n"
    "v_or_b32_e32 v63, v62, v63\n"
    "v_lshlrev_b32_sdwa v101, 1, v101 dst_sel:DWORD dst_unused:UNUSED_PAD src0_sel:DWORD src1_sel:WORD_1\n"
    "v_or_b32_e64 v62, v60, v61\n"
    "v_lshlrev_b32_sdwa v102, 1, v102 dst_sel:DWORD dst_unused:UNUSED_PAD src0_sel:DWORD src1_sel:WORD_1\n"
    "s_add_u32 m0, s47, 4096\n"
    "v_or_b32_e64 v61, v58, v59\n"
    "v_lshlrev_b32_sdwa v103, 1, v103 dst_sel:DWORD dst_unused:UNUSED_PAD src0_sel:DWORD src1_sel:WORD_1\n"
    "ds_read_u16_d16_hi v101, v101 offset:65472\n"
    "s_waitcnt lgkmcnt(14)\n"
    "v_or_b32_e32 v60, v56, v57\n"
    "v_lshlrev_b32_sdwa v46, 1, v46 dst_sel:DWORD dst_unused:UNUSED_PAD src0_sel:DWORD src1_sel:WORD_1\n"
    "s_waitcnt lgkmcnt(3)\n"
    "buffer_load_dwordx4 v120, s[28:31], s49 offen lds\n"
    "v_or_b32_e32 v59, v54, v55\n"
    "v_lshlrev_b32_sdwa v47, 1, v47 dst_sel:DWORD dst_unused:UNUSED_PAD src0_sel:DWORD src1_sel:WORD_1\n"
    "v_or_b32_e32 v58, v52, v53\n"
    "v_or_b32_e32 v57, v50, v51\n"
    "ds_read_u16 v102, v102 offset:65472\n"
    "buffer_load_dwordx4 v120, s[28:31], s49 offen offset:1024 lds\n"
    "s_waitcnt lgkmcnt(3)\n"
    "v_or_b32_e32 v56, v48, v49\n"
    "v_mfma_f32_32x32x16_bf16 a[16:31], v[60:63], a[84:87], a[16:31]\n"
    "s_add_u32 s49, s49, 2048\n"
    "v_lshlrev_b32_sdwa v79, 1, v79 dst_sel:DWORD dst_unused:UNUSED_PAD src0_sel:DWORD src1_sel:WORD_1\n"
    "v_lshlrev_b32_sdwa v78, 1, v78 dst_sel:DWORD dst_unused:UNUSED_PAD src0_sel:DWORD src1_sel:WORD_1\n"
    "v_lshlrev_b32_sdwa v77, 1, v77 dst_sel:DWORD dst_unused:UNUSED_PAD src0_sel:DWORD src1_sel:WORD_1\n"
    "v_mfma_f32_32x32x16_bf16 a[16:31], v[56:59], a[80:83], a[16:31]\n"
    "ds_read_u16_d16_hi v103, v103 offset:65472\n"
    "v_lshlrev_b32_sdwa v76, 1, v76 dst_sel:DWORD dst_unused:UNUSED_PAD src0_sel:DWORD src1_sel:WORD_1\n"
    "v_lshlrev_b32_sdwa v75, 1, v75 dst_sel:DWORD dst_unused:UNUSED_PAD src0_sel:DWORD src1_sel:WORD_1\n"
    "v_lshlrev_b32_sdwa v74, 1, v74 dst_sel:DWORD dst_unused:UNUSED_PAD src0_sel:DWORD src1_sel:WORD_1\n"
    "v_lshlrev_b32_sdwa v73, 1, v73 dst_sel:DWORD dst_unused:UNUSED_PAD src0_sel:DWORD src1_sel:WORD_1\n"
    "v_mfma_f32_32x32x16_bf16 v[48:63], a[72:75], a[56:59], v[16:31]\n"
    "ds_read_u16 v46, v46 offset:65472\n"
    "v_lshlrev_b32_sdwa v72, 1, v72 dst_sel:DWORD dst_unused:UNUSED_PAD src0_sel:DWORD src1_sel:WORD_1\n"
    "v_lshlrev_b32_sdwa v71, 1, v71 dst_sel:DWORD dst_unused:UNUSED_PAD src0_sel:DWORD src1_sel:WORD_1\n"
    "v_lshlrev_b32_sdwa v70, 1, v70 dst_sel:DWORD dst_unused:UNUSED_PAD src0_sel:DWORD src1_sel:WORD_1\n"
    "s_waitcnt vmcnt(4)\n"
    "ds_read_b64_tr_b16 a[80:81], v112\n"
    "v_lshlrev_b32_sdwa v69, 1, v69 dst_sel:DWORD dst_unused:UNUSED_PAD src0_sel:DWORD src1_sel:WORD_1\n"
    "v_mfma_f32_32x32x16_bf16 v[16:31], a[72:75], a[48:51], v[16:31]\n"
    "ds_read_b64_tr_b16 a[82:83], v112 offset:512\n"
    "v_lshlrev_b32_sdwa v68, 1, v68 dst_sel:DWORD dst_unused:UNUSED_PAD src0_sel:DWORD src1_sel:WORD_1\n"
    "ds_read_u16_d16_hi v47, v47 offset:65472\n"
    "ds_read_b64_tr_b16 a[84:85], v112 offset:1024\n"
    "v_lshlrev_b32_sdwa v67, 1, v67 dst_sel:DWORD dst_unused:UNUSED_PAD src0_sel:DWORD src1_sel:WORD_1\n"
    "ds_read_b64_tr_b16 a[86:87], v112 offset:1536\n"
    "v_lshlrev_b32_sdwa v66, 1, v66 dst_sel:DWORD dst_unused:UNUSED_PAD src0_sel:DWORD src1_sel:WORD_1\n"
    "ds_read_b64_tr_b16 a[90:91], v113\n"
    "v_lshlrev_b32_sdwa v65, 1, v65 dst_sel:DWORD dst_unused:UNUSED_PAD src0_sel:DWORD src1_sel:WORD_1\n"
    "v_mfma_f32_32x32x16_bf16 v[16:31], a[76:79], a[52:55], v[16:31]\n"
    "ds_read_b64_tr_b16 a[94:95], v113 offset:32\n"
    "v_lshlrev_b32_sdwa v64, 1, v64 dst_sel:DWORD dst_unused:UNUSED_PAD src0_sel:DWORD src1_sel:WORD_1\n"
    "ds_read_b64_tr_b16 a[126:127], v113 offset:1024\n"
    "ds_read_b64_tr_b16 v[6:7], v113 offset:1056\n"
    "v_rcp_f32_e32 v80, v80\n"
    "ds_read_u16_d16_hi v79, v79 offset:65472\n"
    "v_rcp_f32_e32 v81, v81\n"
    "v_mfma_f32_32x32x16_bf16 v[48:63], a[76:79], a[60:63], v[48:63]\n"
    "v_rcp_f32_e32 v82, v82\n"
    "ds_read_u16 v78, v78 offset:65472\n"
    "v_rcp_f32_e32 v83, v83\n"
    "v_rcp_f32_e32 v84, v84\n"
    "v_mfma_f32_16x16x32_bf16 v[96:99], a[72:75], a[64:67], v[96:99]\n"
    "ds_read_u16_d16_hi v77, v77 offset:65472\n"
    "v_rcp_f32_e64 v85, v85\n"
    "v_mfma_f32_16x16x32_bf16 v[96:99], a[76:79], a[68:71], v[96:99]\n"
    "v_rcp_f32_e32 v86, v86\n"
    "v_rcp_f32_e32 v87, v87\n"
    "ds_read_u16 v76, v76 offset:65472\n"
    "v_rcp_f32_e32 v88, v88\n"
    "v_rcp_f32_e32 v89, v89\n"
    "v_rcp_f32_e32 v90, v90\n"
    "ds_read_u16_d16_hi v75, v75 offset:65472\n"
    "v_rcp_f32_e32 v91, v91\n"
    "v_rcp_f32_e32 v92, v92\n"
    "v_rcp_f32_e32 v93, v93\n"
    "ds_read_u16 v74, v74 offset:65472\n"
    "s_cmp_lt_u32 s44, 4\n"
    "s_cbranch_scc0 Lq6%=\n"
    "s_setprio 0\n"
    "Lq6%=:\n"
    "v_pk_mul_f32 v[32:33], v[32:33], v[80:81]\n"
    "v_pk_mul_f32 v[34:35], v[34:35], v[82:83]\n"
    "v_pk_mul_f32 v[36:37], v[36:37], v[84:85]\n"
    "v_pk_mul_f32 v[38:39], v[38:39], v[86:87]\n"
    "ds_read_u16_d16_hi v73, v73 offset:65472\n"
    "s_mov_b32 m0, s47\n"
    "v_pk_mul_f32 v[40:41], v[40:41], v[88:89]\n"
    "v_pk_mul_f32 v[42:43], v[42:43], v[90:91]\n"
    "s_waitcnt lgkmcnt(7)\n"
    "buffer_load_dwordx4 v116, s[24:27], s48 offen lds\n"
    "v_pk_mul_f32 v[44:45], v[44:45], v[92:93]\n"
    "buffer_load_dwordx4 v117, s[24:27], s48 offen offset:1024 lds\n"
    "s_add_u32 s48, s48, 64\n"
    "v_pk_fma_f32 v[80:81], v[16:17], v[16:17], s[50:51] neg_lo:[0,1,0] neg_hi:[0,1,0]\n"
    "v_pk_fma_f32 v[82:83], v[18:19], v[18:19], s[50:51] neg_lo:[0,1,0] neg_hi:[0,1,0]\n"
    "v_pk_fma_f32 v[84:85], v[20:21], v[20:21], s[50:51] neg_lo:[0,1,0] neg_hi:[0,1,0]\n"
    "ds_read_u16 v72, v72 offset:65472\n"
    "v_pk_fma_f32 v[86:87], v[22:23], v[22:23], s[50:51] neg_lo:[0,1,0] neg_hi:[0,1,0]\n"
    "v_pk_fma_f32 v[88:89], v[24:25], v[24:25], s[50:51] neg_lo:[0,1,0] neg_hi:[0,1,0]\n"
    "v_pk_fma_f32 v[90:91], v[26:27], v[26:27], s[50:51] neg_lo:[0,1,0] neg_hi:[0,1,0]\n"
    "v_pk_fma_f32 v[92:93], v[28:29], v[28:29], s[50:51] neg_lo:[0,1,0] neg_hi:[0,1,0]\n"
    "v_pk_mul_f32 v[80:81], v[80:81], v[16:17]\n"
    "v_pk_mul_f32 v[82:83], v[82:83], v[18:19]\n"
    "ds_read_u16_d16_hi v71, v71 offset:65472\n"
    "v_pk_mul_f32 v[84:85], v[84:85], v[20:21]\n"
    "v_pk_mul_f32 v[86:87], v[86:87], v[22:23]\n"
    "v_pk_mul_f32 v[88:89], v[88:89], v[24:25]\n"
    "v_pk_mul_f32 v[90:91], v[90:91], v[26:27]\n"
    "v_pk_mul_f32 v[92:93], v[92:93], v[28:29]\n"
    "v_cvt_pk_bf16_f32 v32, v32, v33\n"
    "ds_read_u16 v70, v70 offset:65472\n"
    "v_cvt_pk_bf16_f32 v33, v34, v35\n"
    "v_cvt_pk_bf16_f32 v34, v36, v37\n"
    "v_cvt_pk_bf16_f32 v35, v38, v39\n"
    "v_cvt_pk_bf16_f32 v36, v40, v41\n"
    "v_cvt_pk_bf16_f32 v37, v42, v43\n"
    "ds_read_u16_d16_hi v69, v69 offset:65472\n"
    "v_cvt_pk_bf16_f32 v38, v44, v45\n"
    "v_or_b32_e32 v39, v46, v47\n"
    "v_or_b32_e32 v110, v100, v101\n"
    "v_or_b32_e32 v111, v102, v103\n"
    "v_mfma_f32_32x32x16_bf16 a[0:15], v[32:35], a[80:83], a[0:15]\n"
    "v_exp_f32_e32 v80, v80\n"
    "ds_read_u16 v68, v68 offset:65472\n"
    "v_exp_f32_e32 v81, v81\n"
    "v_mfma_f32_32x32x16_bf16 a[0:15], v[36:39], a[84:87], a[0:15]\n"
    "v_exp_f32_e32 v82, v82\n"
    "ds_read_u16_d16_hi v67, v67 offset:65472\n"
    "v_exp_f32_e64 v83, v83\n"
    "v_mfma_f32_16x16x32_bf16 a[32:35], v[108:111], a[88:91], a[32:35]\n"
    "v_exp_f32_e32 v84, v84\n"
    "v_exp_f32_e32 v85, v85\n"
    "ds_read_u16 v66, v66 offset:65472\n"
    "v_mfma_f32_16x16x32_bf16 a[36:39], v[108:111], a[92:95], a[36:39]\n"
    "s_waitcnt vmcnt(4)\n"
    "ds_read_b64_tr_b16 a[72:73], v112 offset:2048\n"
    "v_exp_f32_e32 v86, v86\n"
    "v_mfma_f32_16x16x32_bf16 a[40:43], v[108:111], a[124:127], a[40:43]\n"
    "ds_read_u16_d16_hi v65, v65 offset:65472\n"
    "ds_read_b64_tr_b16 a[74:75], v112 offset:2560\n"
    "v_exp_f32_e64 v87, v87\n"
    "v_mfma_f32_16x16x32_bf16 a[44:47], v[108:111], v[4:7], a[44:47]\n"
    "ds_read_b64_tr_b16 a[76:77], v112 offset:3072\n"
    "v_exp_f32_e64 v88, v88\n"
    "v_mfma_f32_16x16x16bf16_1k v[100:103], a[98:99], a[120:121], 0\n"
    "ds_read_u16 v64, v64 offset:65472\n"
    "ds_read_b64_tr_b16 a[78:79], v112 offset:3584\n"
    "v_exp_f32_e32 v89, v89\n"
    "v_mfma_f32_32x32x8bf16_1k v[32:47], a[98:99], a[112:113], 0\n"
    "v_exp_f32_e32 v90, v90\n"
    "v_exp_f32_e32 v91, v91\n"
    "v_exp_f32_e32 v92, v92\n"
    "v_exp_f32_e32 v93, v93\n"
    "s_nop 2\n"
    "v_mfma_f32_16x16x4bf16_1k v[80:95], a[122:123], a[122:123], v[80:95]\n"
    "v_lshlrev_b32_sdwa v96, 1, v96 dst_sel:DWORD dst_unused:UNUSED_PAD src0_sel:DWORD src1_sel:WORD_1\n"
    "ds_read_u16 v96, v96 offset:65472\n"
    "s_waitcnt lgkmcnt(15)\n"
    "v_or_b32_e32 v79, v78, v79\n"
    "v_lshlrev_b32_sdwa v97, 1, v97 dst_sel:DWORD dst_unused:UNUSED_PAD src0_sel:DWORD src1_sel:WORD_1\n"
    "v_or_b32_e64 v78, v76, v77\n"
    "v_lshlrev_b32_sdwa v98, 1, v98 dst_sel:DWORD dst_unused:UNUSED_PAD src0_sel:DWORD src1_sel:WORD_1\n"
    "s_add_u32 m0, s47, 2048\n"
    "v_or_b32_e64 v77, v74, v75\n"
    "v_lshlrev_b32_sdwa v99, 1, v99 dst_sel:DWORD dst_unused:UNUSED_PAD src0_sel:DWORD src1_sel:WORD_1\n"
    "ds_read_u16_d16_hi v97, v97 offset:65472\n"
    "s_waitcnt lgkmcnt(14)\n"
    "v_or_b32_e32 v76, v72, v73\n"
    "v_lshlrev_b32_sdwa v30, 1, v30 dst_sel:DWORD dst_unused:UNUSED_PAD src0_sel:DWORD src1_sel:WORD_1\n"
    "s_waitcnt lgkmcnt(2)\n"
    "buffer_load_dwordx4 v120, s[28:31], s49 offen lds\n"
    "v_or_b32_e32 v75, v70, v71\n"
    "v_lshlrev_b32_sdwa v31, 1, v31 dst_sel:DWORD dst_unused:UNUSED_PAD src0_sel:DWORD src1_sel:WORD_1\n"
    "v_or_b32_e32 v74, v68, v69\n"
    "v_or_b32_e32 v73, v66, v67\n"
    "ds_read_u16 v98, v98 offset:65472\n"
    "buffer_load_dwordx4 v120, s[28:31], s49 offen offset:1024 lds\n"
    "v_or_b32_e32 v72, v64, v65\n"
    "v_mfma_f32_32x32x16_bf16 a[16:31], v[76:79], a[84:87], a[16:31]\n"
    "s_add_u32 s49, s49, 2048\n"
    "v_lshlrev_b32_sdwa v63, 1, v63 dst_sel:DWORD dst_unused:UNUSED_PAD src0_sel:DWORD src1_sel:WORD_1\n"
    "v_lshlrev_b32_sdwa v62, 1, v62 dst_sel:DWORD dst_unused:UNUSED_PAD src0_sel:DWORD src1_sel:WORD_1\n"
    "v_lshlrev_b32_sdwa v61, 1, v61 dst_sel:DWORD dst_unused:UNUSED_PAD src0_sel:DWORD src1_sel:WORD_1\n"
    "v_mfma_f32_32x32x16_bf16 a[16:31], v[72:75], a[80:83], a[16:31]\n"
    "ds_read_u16_d16_hi v99, v99 offset:65472\n"
    "v_lshlrev_b32_sdwa v60, 1, v60 dst_sel:DWORD dst_unused:UNUSED_PAD src0_sel:DWORD src1_sel:WORD_1\n"
    "v_lshlrev_b32_sdwa v59, 1, v59 dst_sel:DWORD dst_unused:UNUSED_PAD src0_sel:DWORD src1_sel:WORD_1\n"
    "v_lshlrev_b32_sdwa v58, 1, v58 dst_sel:DWORD dst_unused:UNUSED_PAD src0_sel:DWORD src1_sel:WORD_1\n"
    "v_lshlrev_b32_sdwa v57, 1, v57 dst_sel:DWORD dst_unused:UNUSED_PAD src0_sel:DWORD src1_sel:WORD_1\n"
    "v_mfma_f32_32x32x16_bf16 v[64:79], a[72:75], a[56:59], v[32:47]\n"
    "ds_read_u16 v30, v30 offset:65472\n"
    "v_lshlrev_b32_sdwa v56, 1, v56 dst_sel:DWORD dst_unused:UNUSED_PAD src0_sel:DWORD src1_sel:WORD_1\n"
    "v_lshlrev_b32_sdwa v55, 1, v55 dst_sel:DWORD dst_unused:UNUSED_PAD src0_sel:DWORD src1_sel:WORD_1\n"
    "v_lshlrev_b32_sdwa v54, 1, v54 dst_sel:DWORD dst_unused:UNUSED_PAD src0_sel:DWORD src1_sel:WORD_1\n"
    "s_waitcnt vmcnt(4)\n"
    "ds_read_b64_tr_b16 a[80:81], v112 offset:4096\n"
    "v_lshlrev_b32_sdwa v53, 1, v53 dst_sel:DWORD dst_unused:UNUSED_PAD src0_sel:DWORD src1_sel:WORD_1\n"
    "v_mfma_f32_32x32x16_bf16 v[32:47], a[72:75], a[48:51], v[32:47]\n"
    "ds_read_b64_tr_b16 a[82:83], v112 offset:4608\n"
    "v_lshlrev_b32_sdwa v52, 1, v52 dst_sel:DWORD dst_unused:UNUSED_PAD src0_sel:DWORD src1_sel:WORD_1\n"
    "ds_read_u16_d16_hi v31, v31 offset:65472\n"
    "ds_read_b64_tr_b16 a[84:85], v112 offset:5120\n"
    "v_lshlrev_b32_sdwa v51, 1, v51 dst_sel:DWORD dst_unused:UNUSED_PAD src0_sel:DWORD src1_sel:WORD_1\n"
    "ds_read_b64_tr_b16 a[86:87], v112 offset:5632\n"
    "v_lshlrev_b32_sdwa v50, 1, v50 dst_sel:DWORD dst_unused:UNUSED_PAD src0_sel:DWORD src1_sel:WORD_1\n"
    "ds_read_b64_tr_b16 a[88:89], v113 offset:4096\n"
    "v_lshlrev_b32_sdwa v49, 1, v49 dst_sel:DWORD dst_unused:UNUSED_PAD src0_sel:DWORD src1_sel:WORD_1\n"
    "v_mfma_f32_32x32x16_bf16 v[32:47], a[76:79], a[52:55], v[32:47]\n"
    "ds_read_b64_tr_b16 a[92:93], v113 offset:4128\n"
    "v_lshlrev_b32_sdwa v48, 1, v48 dst_sel:DWORD dst_unused:UNUSED_PAD src0_sel:DWORD src1_sel:WORD_1\n"
    "ds_read_b64_tr_b16 a[124:125], v113 offset:5120\n"
    "ds_read_b64_tr_b16 v[4:5], v113 offset:5152\n"
    "v_rcp_f32_e32 v80, v80\n"
    "ds_read_u16_d16_hi v63, v63 offset:65472\n"
    "v_rcp_f32_e32 v81, v81\n"
    "v_mfma_f32_32x32x16_bf16 v[64:79], a[76:79], a[60:63], v[64:79]\n"
    "v_rcp_f32_e64 v82, v82\n"
    "ds_read_u16 v62, v62 offset:65472\n"
    "v_rcp_f32_e32 v83, v83\n"
    "v_rcp_f32_e32 v84, v84\n"
    "v_mfma_f32_16x16x32_bf16 v[100:103], a[72:75], a[64:67], v[100:103]\n"
    "ds_read_u16_d16_hi v61, v61 offset:65472\n"
    "v_rcp_f32_e64 v85, v85\n"
    "v_mfma_f32_16x16x32_bf16 v[100:103], a[76:79], a[68:71], v[100:103]\n"
    "v_rcp_f32_e32 v86, v86\n"
    "v_rcp_f32_e32 v87, v87\n"
    "ds_read_u16 v60, v60 offset:65472\n"
    "v_rcp_f32_e32 v88, v88\n"
    "v_rcp_f32_e32 v89, v89\n"
    "v_rcp_f32_e32 v90, v90\n"
    "ds_read_u16_d16_hi v59, v59 offset:65472\n"
    "v_rcp_f32_e32 v91, v91\n"
    "v_rcp_f32_e32 v92, v92\n"
    "v_rcp_f32_e32 v93, v93\n"
    "ds_read_u16 v58, v58 offset:65472\n"
    "v_pk_mul_f32 v[16:17], v[16:17], v[80:81]\n"
    "v_pk_mul_f32 v[18:19], v[18:19], v[82:83]\n"
    "v_pk_mul_f32 v[20:21], v[20:21], v[84:85]\n"
    "v_pk_mul_f32 v[22:23], v[22:23], v[86:87]\n"
    "ds_read_u16_d16_hi v57, v57 offset:65472\n"
    "s_add_u32 m0, s47, 4096\n"
    "v_pk_mul_f32 v[24:25], v[24:25], v[88:89]\n"
    "v_pk_mul_f32 v[26:27], v[26:27], v[90:91]\n"
    "s_waitcnt lgkmcnt(7)\n"
    "buffer_load_dwordx4 v116, s[24:27], s48 offen lds\n"
    "v_pk_mul_f32 v[28:29], v[28:29], v[92:93]\n"
    "buffer_load_dwordx4 v117, s[24:27], s48 offen offset:1024 lds\n"
    "s_add_u32 s48, s48, 64\n"
    "v_pk_fma_f32 v[80:81], v[32:33], v[32:33], s[50:51] neg_lo:[0,1,0] neg_hi:[0,1,0]\n"
    "v_pk_fma_f32 v[82:83], v[34:35], v[34:35], s[50:51] neg_lo:[0,1,0] neg_hi:[0,1,0]\n"
    "v_pk_fma_f32 v[84:85], v[36:37], v[36:37], s[50:51] neg_lo:[0,1,0] neg_hi:[0,1,0]\n"
    "ds_read_u16 v56, v56 offset:65472\n"
    "v_pk_fma_f32 v[86:87], v[38:39], v[38:39], s[50:51] neg_lo:[0,1,0] neg_hi:[0,1,0]\n"
    "v_pk_fma_f32 v[88:89], v[40:41], v[40:41], s[50:51] neg_lo:[0,1,0] neg_hi:[0,1,0]\n"
    "v_pk_fma_f32 v[90:91], v[42:43], v[42:43], s[50:51] neg_lo:[0,1,0] neg_hi:[0,1,0]\n"
    "v_pk_fma_f32 v[92:93], v[44:45], v[44:45], s[50:51] neg_lo:[0,1,0] neg_hi:[0,1,0]\n"
    "v_pk_mul_f32 v[80:81], v[80:81], v[32:33]\n"
    "v_pk_mul_f32 v[82:83], v[82:83], v[34:35]\n"
    "ds_read_u16_d16_hi v55, v55 offset:65472\n"
    "v_pk_mul_f32 v[84:85], v[84:85], v[36:37]\n"
    "v_pk_mul_f32 v[86:87], v[86:87], v[38:39]\n"
    "v_pk_mul_f32 v[88:89], v[88:89], v[40:41]\n"
    "v_pk_mul_f32 v[90:91], v[90:91], v[42:43]\n"
    "v_pk_mul_f32 v[92:93], v[92:93], v[44:45]\n"
    "v_cvt_pk_bf16_f32 v16, v16, v17\n"
    "ds_read_u16 v54, v54 offset:65472\n"
    "v_cvt_pk_bf16_f32 v17, v18, v19\n"
    "v_cvt_pk_bf16_f32 v18, v20, v21\n"
    "v_cvt_pk_bf16_f32 v19, v22, v23\n"
    "v_cvt_pk_bf16_f32 v20, v24, v25\n"
    "v_cvt_pk_bf16_f32 v21, v26, v27\n"
    "ds_read_u16_d16_hi v53, v53 offset:65472\n"
    "v_cvt_pk_bf16_f32 v22, v28, v29\n"
    "v_or_b32_e32 v23, v30, v31\n"
    "v_or_b32_e32 v108, v96, v97\n"
    "v_or_b32_e32 v109, v98, v99\n"
    "v_mfma_f32_32x32x16_bf16 a[0:15], v[16:19], a[80:83], a[0:15]\n"
    "v_exp_f32_e32 v80, v80\n"
    "ds_read_u16 v52, v52 offset:65472\n"
    "v_exp_f32_e32 v81, v81\n"
    "v_mfma_f32_32x32x16_bf16 a[0:15], v[20:23], a[84:87], a[0:15]\n"
    "v_exp_f32_e32 v82, v82\n"
    "ds_read_u16_d16_hi v51, v51 offset:65472\n"
    "v_exp_f32_e32 v83, v83\n"
    "v_exp_f32_e32 v84, v84\n"
    "v_exp_f32_e32 v85, v85\n"
    "ds_read_u16 v50, v50 offset:65472\n"
    "s_waitcnt vmcnt(4)\n"
    "ds_read_b64_tr_b16 a[72:73], v112\n"
    "v_exp_f32_e32 v86, v86\n"
    "ds_read_b64_tr_b16 a[74:75], v112 offset:512\n"
    "v_exp_f32_e32 v87, v87\n"
    "ds_read_b64_tr_b16 a[76:77], v112 offset:1024\n"
    "v_exp_f32_e64 v88, v88\n"
    "ds_read_u16_d16_hi v49, v49 offset:65472\n"
    "v_mfma_f32_16x16x16bf16_1k v[96:99], a[100:101], a[114:115], 0\n"
    "ds_read_b64_tr_b16 a[78:79], v112 offset:1536\n"
    "v_exp_f32_e32 v89, v89\n"
    "v_mfma_f32_32x32x8bf16_1k v[16:31], a[100:101], a[106:107], 0\n"
    "v_exp_f32_e32 v90, v90\n"
    "ds_read_u16 v48, v48 offset:65472\n"
    "v_exp_f32_e32 v91, v91\n"
    "v_exp_f32_e32 v92, v92\n"
    "v_exp_f32_e32 v93, v93\n"
    "s_nop 2\n"
    "v_mfma_f32_16x16x4bf16_1k v[80:95], a[122:123], a[122:123], v[80:95]\n"
    "v_lshlrev_b32_sdwa v100, 1, v100 dst_sel:DWORD dst_unused:UNUSED_PAD src0_sel:DWORD src1_sel:WORD_1\n"
    "ds_read_u16 v100, v100 offset:65472\n"
    "s_waitcnt lgkmcnt(15)\n"
    "v_or_b32_e32 v63, v62, v63\n"
    "v_lshlrev_b32_sdwa v101, 1, v101 dst_sel:DWORD dst_unused:UNUSED_PAD src0_sel:DWORD src1_sel:WORD_1\n"
    "v_or_b32_e64 v62, v60, v61\n"
    "v_lshlrev_b32_sdwa v102, 1, v102 dst_sel:DWORD dst_unused:UNUSED_PAD src0_sel:DWORD src1_sel:WORD_1\n"
    "s_mov_b32 m0, s47\n"
    "v_or_b32_e32 v61, v58, v59\n"
    "v_lshlrev_b32_sdwa v103, 1, v103 dst_sel:DWORD dst_unused:UNUSED_PAD src0_sel:DWORD src1_sel:WORD_1\n"
    "ds_read_u16_d16_hi v101, v101 offset:65472\n"
    "s_waitcnt lgkmcnt(14)\n"
    "v_or_b32_e32 v60, v56, v57\n"
    "v_lshlrev_b32_sdwa v46, 1, v46 dst_sel:DWORD dst_unused:UNUSED_PAD src0_sel:DWORD src1_sel:WORD_1\n"
    "s_waitcnt lgkmcnt(3)\n"
    "buffer_load_dwordx4 v120, s[28:31], s49 offen lds\n"
    "v_or_b32_e32 v59, v54, v55\n"
    "v_lshlrev_b32_sdwa v47, 1, v47 dst_sel:DWORD dst_unused:UNUSED_PAD src0_sel:DWORD src1_sel:WORD_1\n"
    "v_or_b32_e32 v58, v52, v53\n"
    "v_or_b32_e32 v57, v50, v51\n"
    "ds_read_u16 v102, v102 offset:65472\n"
    "buffer_load_dwordx4 v120, s[28:31], s49 offen offset:1024 lds\n"
    "s_waitcnt lgkmcnt(3)\n"
    "v_or_b32_e32 v56, v48, v49\n"
    "v_mfma_f32_32x32x16_bf16 a[16:31], v[60:63], a[84:87], a[16:31]\n"
    "s_add_u32 s49, s49, 2048\n"
    "v_lshlrev_b32_sdwa v79, 1, v79 dst_sel:DWORD dst_unused:UNUSED_PAD src0_sel:DWORD src1_sel:WORD_1\n"
    "v_lshlrev_b32_sdwa v78, 1, v78 dst_sel:DWORD dst_unused:UNUSED_PAD src0_sel:DWORD src1_sel:WORD_1\n"
    "v_lshlrev_b32_sdwa v77, 1, v77 dst_sel:DWORD dst_unused:UNUSED_PAD src0_sel:DWORD src1_sel:WORD_1\n"
    "v_mfma_f32_32x32x16_bf16 a[16:31], v[56:59], a[80:83], a[16:31]\n"
    "ds_read_u16_d16_hi v103, v103 offset:65472\n"
    "v_lshlrev_b32_sdwa v76, 1, v76 dst_sel:DWORD dst_unused:UNUSED_PAD src0_sel:DWORD src1_sel:WORD_1\n"
    "v_lshlrev_b32_sdwa v75, 1, v75 dst_sel:DWORD dst_unused:UNUSED_PAD src0_sel:DWORD src1_sel:WORD_1\n"
    "v_lshlrev_b32_sdwa v74, 1, v74 dst_sel:DWORD dst_unused:UNUSED_PAD src0_sel:DWORD src1_sel:WORD_1\n"
    "v_lshlrev_b32_sdwa v73, 1, v73 dst_sel:DWORD dst_unused:UNUSED_PAD src0_sel:DWORD src1_sel:WORD_1\n"
    "v_mfma_f32_32x32x16_bf16 v[48:63], a[72:75], a[56:59], v[16:31]\n"
    "ds_read_u16 v46, v46 offset:65472\n"
    "v_lshlrev_b32_sdwa v72, 1, v72 dst_sel:DWORD dst_unused:UNUSED_PAD src0_sel:DWORD src1_sel:WORD_1\n"
    "v_lshlrev_b32_sdwa v71, 1, v71 dst_sel:DWORD dst_unused:UNUSED_PAD src0_sel:DWORD src1_sel:WORD_1\n"
    "v_lshlrev_b32_sdwa v70, 1, v70 dst_sel:DWORD dst_unused:UNUSED_PAD src0_sel:DWORD src1_sel:WORD_1\n"
    "s_waitcnt vmcnt(4)\n"
    "ds_read_b64_tr_b16 a[80:81], v112 offset:2048\n"
    "v_lshlrev_b32_sdwa v69, 1, v69 dst_sel:DWORD dst_unused:UNUSED_PAD src0_sel:DWORD src1_sel:WORD_1\n"
    "v_mfma_f32_32x32x16_bf16 v[16:31], a[72:75], a[48:51], v[16:31]\n"
    "ds_read_b64_tr_b16 a[82:83], v112 offset:2560\n"
    "v_lshlrev_b32_sdwa v68, 1, v68 dst_sel:DWORD dst_unused:UNUSED_PAD src0_sel:DWORD src1_sel:WORD_1\n"
    "ds_read_u16_d16_hi v47, v47 offset:65472\n"
    "ds_read_b64_tr_b16 a[84:85], v112 offset:3072\n"
    "v_lshlrev_b32_sdwa v67, 1, v67 dst_sel:DWORD dst_unused:UNUSED_PAD src0_sel:DWORD src1_sel:WORD_1\n"
    "ds_read_b64_tr_b16 a[86:87], v112 offset:3584\n"
    "v_lshlrev_b32_sdwa v66, 1, v66 dst_sel:DWORD dst_unused:UNUSED_PAD src0_sel:DWORD src1_sel:WORD_1\n"
    "ds_read_b64_tr_b16 a[90:91], v113 offset:2048\n"
    "v_lshlrev_b32_sdwa v65, 1, v65 dst_sel:DWORD dst_unused:UNUSED_PAD src0_sel:DWORD src1_sel:WORD_1\n"
    "v_mfma_f32_32x32x16_bf16 v[16:31], a[76:79], a[52:55], v[16:31]\n"
    "ds_read_b64_tr_b16 a[94:95], v113 offset:2080\n"
    "v_lshlrev_b32_sdwa v64, 1, v64 dst_sel:DWORD dst_unused:UNUSED_PAD src0_sel:DWORD src1_sel:WORD_1\n"
    "ds_read_b64_tr_b16 a[126:127], v113 offset:3072\n"
    "ds_read_b64_tr_b16 v[6:7], v113 offset:3104\n"
    "v_rcp_f32_e32 v80, v80\n"
    "ds_read_u16_d16_hi v79, v79 offset:65472\n"
    "v_rcp_f32_e32 v81, v81\n"
    "v_mfma_f32_32x32x16_bf16 v[48:63], a[76:79], a[60:63], v[48:63]\n"
    "v_rcp_f32_e32 v82, v82\n"
    "ds_read_u16 v78, v78 offset:65472\n"
    "v_rcp_f32_e32 v83, v83\n"
    "v_rcp_f32_e32 v84, v84\n"
    "v_mfma_f32_16x16x32_bf16 v[96:99], a[72:75], a[64:67], v[96:99]\n"
    "ds_read_u16_d16_hi v77, v77 offset:65472\n"
    "v_rcp_f32_e64 v85, v85\n"
    "v_mfma_f32_16x16x32_bf16 v[96:99], a[76:79], a[68:71], v[96:99]\n"
    "v_rcp_f32_e32 v86, v86\n"
    "v_rcp_f32_e32 v87, v87\n"
    "ds_read_u16 v76, v76 offset:65472\n"
    "v_rcp_f32_e32 v88, v88\n"
    "v_rcp_f32_e32 v89, v89\n"
    "v_rcp_f32_e64 v90, v90\n"
    "ds_read_u16_d16_hi v75, v75 offset:65472\n"
    "v_rcp_f32_e32 v91, v91\n"
    "v_rcp_f32_e32 v92, v92\n"
    "v_rcp_f32_e32 v93, v93\n"
    "ds_read_u16 v74, v74 offset:65472\n"
    "s_cmp_lt_u32 s44, 4\n"
    "s_cbranch_scc0 Lq8%=\n"
    "s_setprio 2\n"
    "Lq8%=:\n"
    "v_pk_mul_f32 v[32:33], v[32:33], v[80:81]\n"
    "v_pk_mul_f32 v[34:35], v[34:35], v[82:83]\n"
    "v_pk_mul_f32 v[36:37], v[36:37], v[84:85]\n"
    "v_pk_mul_f32 v[38:39], v[38:39], v[86:87]\n"
    "ds_read_u16_d16_hi v73, v73 offset:65472\n"
    "s_add_u32 m0, s47, 2048\n"
    "v_pk_mul_f32 v[40:41], v[40:41], v[88:89]\n"
    "v_pk_mul_f32 v[42:43], v[42:43], v[90:91]\n"
    "s_waitcnt lgkmcnt(7)\n"
    "buffer_load_dwordx4 v116, s[24:27], s48 offen lds\n"
    "v_pk_mul_f32 v[44:45], v[44:45], v[92:93]\n"
    "buffer_load_dwordx4 v117, s[24:27], s48 offen offset:1024 lds\n"
    "s_add_u32 s48, s48, 64\n"
    "v_pk_fma_f32 v[80:81], v[16:17], v[16:17], s[50:51] neg_lo:[0,1,0] neg_hi:[0,1,0]\n"
    "v_pk_fma_f32 v[82:83], v[18:19], v[18:19], s[50:51] neg_lo:[0,1,0] neg_hi:[0,1,0]\n"
    "v_pk_fma_f32 v[84:85], v[20:21], v[20:21], s[50:51] neg_lo:[0,1,0] neg_hi:[0,1,0]\n"
    "ds_read_u16 v72, v72 offset:65472\n"
    "v_pk_fma_f32 v[86:87], v[22:23], v[22:23], s[50:51] neg_lo:[0,1,0] neg_hi:[0,1,0]\n"
    "v_pk_fma_f32 v[88:89], v[24:25], v[24:25], s[50:51] neg_lo:[0,1,0] neg_hi:[0,1,0]\n"
    "v_pk_fma_f32 v[90:91], v[26:27], v[26:27], s[50:51] neg_lo:[0,1,0] neg_hi:[0,1,0]\n"
    "v_pk_fma_f32 v[92:93], v[28:29], v[28:29], s[50:51] neg_lo:[0,1,0] neg_hi:[0,1,0]\n"
    "v_pk_mul_f32 v[80:81], v[80:81], v[16:17]\n"
    "v_pk_mul_f32 v[82:83], v[82:83], v[18:19]\n"
    "ds_read_u16_d16_hi v71, v71 offset:65472\n"
    "v_pk_mul_f32 v[84:85], v[84:85], v[20:21]\n"
    "v_pk_mul_f32 v[86:87], v[86:87], v[22:23]\n"
    "v_pk_mul_f32 v[88:89], v[88:89], v[24:25]\n"
    "v_pk_mul_f32 v[90:91], v[90:91], v[26:27]\n"
    "v_pk_mul_f32 v[92:93], v[92:93], v[28:29]\n"
    "v_cvt_pk_bf16_f32 v32, v32, v33\n"
    "ds_read_u16 v70, v70 offset:65472\n"
    "v_cvt_pk_bf16_f32 v33, v34, v35\n"
    "v_cvt_pk_bf16_f32 v34, v36, v37\n"
    "v_cvt_pk_bf16_f32 v35, v38, v39\n"
    "v_cvt_pk_bf16_f32 v36, v40, v41\n"
    "v_cvt_pk_bf16_f32 v37, v42, v43\n"
    "ds_read_u16_d16_hi v69, v69 offset:65472\n"
    "v_cvt_pk_bf16_f32 v38, v44, v45\n"
    "v_or_b32_e32 v39, v46, v47\n"
    "v_or_b32_e32 v110, v100, v101\n"
    "v_or_b32_e32 v111, v102, v103\n"
    "v_mfma_f32_32x32x16_bf16 a[0:15], v[32:35], a[80:83], a[0:15]\n"
    "v_exp_f32_e32 v80, v80\n"
    "ds_read_u16 v68, v68 offset:65472\n"
    "v_exp_f32_e32 v81, v81\n"
    "v_mfma_f32_32x32x16_bf16 a[0:15], v[36:39], a[84:87], a[0:15]\n"
    "v_exp_f32_e32 v82, v82\n"
    "ds_read_u16_d16_hi v67, v67 offset:65472\n"
    "v_exp_f32_e64 v83, v83\n"
    "v_mfma_f32_16x16x32_bf16 a[32:35], v[108:111], a[88:91], a[32:35]\n"
    "v_exp_f32_e32 v84, v84\n"
    "v_exp_f32_e32 v85, v85\n"
    "ds_read_u16 v66, v66 offset:65472\n"
    "v_mfma_f32_16x16x32_bf16 a[36:39], v[108:111], a[92:95], a[36:39]\n"
    "s_waitcnt vmcnt(4)\n"
    "ds_read_b64_tr_b16 a[72:73], v112 offset:4096\n"
    "v_exp_f32_e32 v86, v86\n"
    "v_mfma_f32_16x16x32_bf16 a[40:43], v[108:111], a[124:127], a[40:43]\n"
    "ds_read_u16_d16_hi v65, v65 offset:65472\n"
    "ds_read_b64_tr_b16 a[74:75], v112 offset:4608\n"
    "v_exp_f32_e64 v87, v87\n"
    "v_mfma_f32_16x16x32_bf16 a[44:47], v[108:111], v[4:7], a[44:47]\n"
    "ds_read_b64_tr_b16 a[76:77], v112 offset:5120\n"
    "v_exp_f32_e64 v88, v88\n"
    "v_mfma_f32_16x16x16bf16_1k v[100:103], a[100:101], a[116:117], 0\n"
    "ds_read_u16 v64, v64 offset:65472\n"
    "ds_read_b64_tr_b16 a[78:79], v112 offset:5632\n"
    "v_exp_f32_e32 v89, v89\n"
    "v_mfma_f32_32x32x8bf16_1k v[32:47], a[100:101], a[108:109], 0\n"
    "v_exp_f32_e32 v90, v90\n"
    "v_exp_f32_e32 v91, v91\n"
    "v_exp_f32_e32 v92, v92\n"
    "v_exp_f32_e32 v93, v93\n"
    "s_nop 2\n"
    "v_mfma_f32_16x16x4bf16_1k v[80:95], a[122:123], a[122:123], v[80:95]\n"
    "v_lshlrev_b32_sdwa v96, 1, v96 dst_sel:DWORD dst_unused:UNUSED_PAD src0_sel:DWORD src1_sel:WORD_1\n"
    "ds_read_u16 v96, v96 offset:65472\n"
    "s_waitcnt lgkmcnt(15)\n"
    "v_or_b32_e32 v79, v78, v79\n"
    "v_lshlrev_b32_sdwa v97, 1, v97 dst_sel:DWORD dst_unused:UNUSED_PAD src0_sel:DWORD src1_sel:WORD_1\n"
    "v_or_b32_e64 v78, v76, v77\n"
    "v_lshlrev_b32_sdwa v98, 1, v98 dst_sel:DWORD dst_unused:UNUSED_PAD src0_sel:DWORD src1_sel:WORD_1\n"
    "s_add_u32 m0, s47, 4096\n"
    "v_or_b32_e64 v77, v74, v75\n"
    "v_lshlrev_b32_sdwa v99, 1, v99 dst_sel:DWORD dst_unused:UNUSED_PAD src0_sel:DWORD src1_sel:WORD_1\n"
    "ds_read_u16_d16_hi v97, v97 offset:65472\n"
    "s_waitcnt lgkmcnt(14)\n"
    "v_or_b32_e32 v76, v72, v73\n"
    "v_lshlrev_b32_sdwa v30, 1, v30 dst_sel:DWORD dst_unused:UNUSED_PAD src0_sel:DWORD src1_sel:WORD_1\n"
    "s_waitcnt lgkmcnt(2)\n"
    "buffer_load_dwordx4 v120, s[28:31], s49 offen lds\n"
    "v_or_b32_e32 v75, v70, v71\n"
    "v_lshlrev_b32_sdwa v31, 1, v31 dst_sel:DWORD dst_unused:UNUSED_PAD src0_sel:DWORD src1_sel:WORD_1\n"
    "v_or_b32_e32 v74, v68, v69\n"
    "v_or_b32_e32 v73, v66, v67\n"
    "ds_read_u16 v98, v98 offset:65472\n"
    "buffer_load_dwordx4 v120, s[28:31], s49 offen offset:1024 lds\n"
    "v_or_b32_e32 v72, v64, v65\n"
    "v_mfma_f32_32x32x16_bf16 a[16:31], v[76:79], a[84:87], a[16:31]\n"
    "s_add_u32 s49, s49, 2048\n"
    "v_lshlrev_b32_sdwa v63, 1, v63 dst_sel:DWORD dst_unused:UNUSED_PAD src0_sel:DWORD src1_sel:WORD_1\n"
    "v_lshlrev_b32_sdwa v62, 1, v62 dst_sel:DWORD dst_unused:UNUSED_PAD src0_sel:DWORD src1_sel:WORD_1\n"
    "v_lshlrev_b32_sdwa v61, 1, v61 dst_sel:DWORD dst_unused:UNUSED_PAD src0_sel:DWORD src1_sel:WORD_1\n"
    "v_mfma_f32_32x32x16_bf16 a[16:31], v[72:75], a[80:83], a[16:31]\n"
    "ds_read_u16_d16_hi v99, v99 offset:65472\n"
    "v_lshlrev_b32_sdwa v60, 1, v60 dst_sel:DWORD dst_unused:UNUSED_PAD src0_sel:DWORD src1_sel:WORD_1\n"
    "v_lshlrev_b32_sdwa v59, 1, v59 dst_sel:DWORD dst_unused:UNUSED_PAD src0_sel:DWORD src1_sel:WORD_1\n"
    "v_lshlrev_b32_sdwa v58, 1, v58 dst_sel:DWORD dst_unused:UNUSED_PAD src0_sel:DWORD src1_sel:WORD_1\n"
    "v_lshlrev_b32_sdwa v57, 1, v57 dst_sel:DWORD dst_unused:UNUSED_PAD src0_sel:DWORD src1_sel:WORD_1\n"
    "v_mfma_f32_32x32x16_bf16 v[64:79], a[72:75], a[56:59], v[32:47]\n"
    "ds_read_u16 v30, v30 offset:65472\n"
    "v_lshlrev_b32_sdwa v56, 1, v56 dst_sel:DWORD dst_unused:UNUSED_PAD src0_sel:DWORD src1_sel:WORD_1\n"
    "v_lshlrev_b32_sdwa v55, 1, v55 dst_sel:DWORD dst_unused:UNUSED_PAD src0_sel:DWORD src1_sel:WORD_1\n"
    "v_lshlrev_b32_sdwa v54, 1, v54 dst_sel:DWORD dst_unused:UNUSED_PAD src0_sel:DWORD src1_sel:WORD_1\n"
    "s_waitcnt vmcnt(4)\n"
    "ds_read_b64_tr_b16 a[80:81], v112\n"
    "v_lshlrev_b32_sdwa v53, 1, v53 dst_sel:DWORD dst_unused:UNUSED_PAD src0_sel:DWORD src1_sel:WORD_1\n"
    "v_mfma_f32_32x32x16_bf16 v[32:47], a[72:75], a[48:51], v[32:47]\n"
    "ds_read_b64_tr_b16 a[82:83], v112 offset:512\n"
    "v_lshlrev_b32_sdwa v52, 1, v52 dst_sel:DWORD dst_unused:UNUSED_PAD src0_sel:DWORD src1_sel:WORD_1\n"
    "ds_read_u16_d16_hi v31, v31 offset:65472\n"
    "ds_read_b64_tr_b16 a[84:85], v112 offset:1024\n"
    "v_lshlrev_b32_sdwa v51, 1, v51 dst_sel:DWORD dst_unused:UNUSED_PAD src0_sel:DWORD src1_sel:WORD_1\n"
    "ds_read_b64_tr_b16 a[86:87], v112 offset:1536\n"
    "v_lshlrev_b32_sdwa v50, 1, v50 dst_sel:DWORD dst_unused:UNUSED_PAD src0_sel:DWORD src1_sel:WORD_1\n"
    "ds_read_b64_tr_b16 a[88:89], v113\n"
    "v_lshlrev_b32_sdwa v49, 1, v49 dst_sel:DWORD dst_unused:UNUSED_PAD src0_sel:DWORD src1_sel:WORD_1\n"
    "v_mfma_f32_32x32x16_bf16 v[32:47], a[76:79], a[52:55], v[32:47]\n"
    "ds_read_b64_tr_b16 a[92:93], v113 offset:32\n"
    "v_lshlrev_b32_sdwa v48, 1, v48 dst_sel:DWORD dst_unused:UNUSED_PAD src0_sel:DWORD src1_sel:WORD_1\n"
    "ds_read_b64_tr_b16 a[124:125], v113 offset:1024\n"
    "ds_read_b64_tr_b16 v[4:5], v113 offset:1056\n"
    "v_rcp_f32_e32 v80, v80\n"
    "ds_read_u16_d16_hi v63, v63 offset:65472\n"
    "v_rcp_f32_e32 v81, v81\n"
    "v_mfma_f32_32x32x16_bf16 v[64:79], a[76:79], a[60:63], v[64:79]\n"
    "v_rcp_f32_e64 v82, v82\n"
    "ds_read_u16 v62, v62 offset:65472\n"
    "v_rcp_f32_e32 v83, v83\n"
    "v_rcp_f32_e32 v84, v84\n"
    "v_mfma_f32_16x16x32_bf16 v[100:103], a[72:75], a[64:67], v[100:103]\n"
    "ds_read_u16_d16_hi v61, v61 offset:65472\n"
    "v_rcp_f32_e64 v85, v85\n"
    "v_mfma_f32_16x16x32_bf16 v[100:103], a[76:79], a[68:71], v[100:103]\n"
    "v_rcp_f32_e32 v86, v86\n"
    "v_rcp_f32_e32 v87, v87\n"
    "ds_read_u16 v60, v60 offset:65472\n"
    "v_rcp_f32_e32 v88, v88\n"
    "v_rcp_f32_e32 v89, v89\n"
    "v_rcp_f32_e64 v90, v90\n"
    "ds_read_u16_d16_hi v59, v59 offset:65472\n"
    "v_rcp_f32_e32 v91, v91\n"
    "v_rcp_f32_e32 v92, v92\n"
    "v_rcp_f32_e32 v93, v93\n"
    "ds_read_u16 v58, v58 offset:65472\n"
    "v_pk_mul_f32 v[16:17], v[16:17], v[80:81]\n"
    "v_pk_mul_f32 v[18:19], v[18:19], v[82:83]\n"
    "v_pk_mul_f32 v[20:21], v[20:21], v[84:85]\n"
    "v_pk_mul_f32 v[22:23], v[22:23], v[86:87]\n"
    "ds_read_u16_d16_hi v57, v57 offset:65472\n"
    "s_mov_b32 m0, s47\n"
    "v_pk_mul_f32 v[24:25], v[24:25], v[88:89]\n"
    "v_pk_mul_f32 v[26:27], v[26:27], v[90:91]\n"
    "s_waitcnt lgkmcnt(7)\n"
    "buffer_load_dwordx4 v116, s[24:27], s48 offen lds\n"
    "v_pk_mul_f32 v[28:29], v[28:29], v[92:93]\n"
    "buffer_load_dwordx4 v117, s[24:27], s48 offen offset:1024 lds\n"
    "s_add_u32 s48, s48, 64\n"
    "v_pk_fma_f32 v[80:81], v[32:33], v[32:33], s[50:51] neg_lo:[0,1,0] neg_hi:[0,1,0]\n"
    "v_pk_fma_f32 v[82:83], v[34:35], v[34:35], s[50:51] neg_lo:[0,1,0] neg_hi:[0,1,0]\n"
    "v_pk_fma_f32 v[84:85], v[36:37], v[36:37], s[50:51] neg_lo:[0,1,0] neg_hi:[0,1,0]\n"
    "ds_read_u16 v56, v56 offset:65472\n"
    "v_pk_fma_f32 v[86:87], v[38:39], v[38:39], s[50:51] neg_lo:[0,1,0] neg_hi:[0,1,0]\n"
    "v_pk_fma_f32 v[88:89], v[40:41], v[40:41], s[50:51] neg_lo:[0,1,0] neg_hi:[0,1,0]\n"
    "v_pk_fma_f32 v[90:91], v[42:43], v[42:43], s[50:51] neg_lo:[0,1,0] neg_hi:[0,1,0]\n"
    "v_pk_fma_f32 v[92:93], v[44:45], v[44:45], s[50:51] neg_lo:[0,1,0] neg_hi:[0,1,0]\n"
    "v_pk_mul_f32 v[80:81], v[80:81], v[32:33]\n"
    "v_pk_mul_f32 v[82:83], v[82:83], v[34:35]\n"
    "ds_read_u16_d16_hi v55, v55 offset:65472\n"
    "v_pk_mul_f32 v[84:85], v[84:85], v[36:37]\n"
    "v_pk_mul_f32 v[86:87], v[86:87], v[38:39]\n"
    "v_pk_mul_f32 v[88:89], v[88:89], v[40:41]\n"
    "v_pk_mul_f32 v[90:91], v[90:91], v[42:43]\n"
    "v_pk_mul_f32 v[92:93], v[92:93], v[44:45]\n"
    "v_cvt_pk_bf16_f32 v16, v16, v17\n"
    "ds_read_u16 v54, v54 offset:65472\n"
    "v_cvt_pk_bf16_f32 v17, v18, v19\n"
    "v_cvt_pk_bf16_f32 v18, v20, v21\n"
    "v_cvt_pk_bf16_f32 v19, v22, v23\n"
    "v_cvt_pk_bf16_f32 v20, v24, v25\n"
    "v_cvt_pk_bf16_f32 v21, v26, v27\n"
    "ds_read_u16_d16_hi v53, v53 offset:65472\n"
    "v_cvt_pk_bf16_f32 v22, v28, v29\n"
    "v_or_b32_e32 v23, v30, v31\n"
    "v_or_b32_e32 v108, v96, v97\n"
    "v_or_b32_e32 v109, v98, v99\n"
    "v_mfma_f32_32x32x16_bf16 a[0:15], v[16:19], a[80:83], a[0:15]\n"
    "v_exp_f32_e32 v80, v80\n"
    "ds_read_u16 v52, v52 offset:65472\n"
    "v_exp_f32_e32 v81, v81\n"
    "v_mfma_f32_32x32x16_bf16 a[0:15], v[20:23], a[84:87], a[0:15]\n"
    "v_exp_f32_e32 v82, v82\n"
    "ds_read_u16_d16_hi v51, v51 offset:65472\n"
    "v_exp_f32_e32 v83, v83\n"
    "v_exp_f32_e32 v84, v84\n"
    "v_exp_f32_e32 v85, v85\n"
    "ds_read_u16 v50, v50 offset:65472\n"
    "s_waitcnt vmcnt(4)\n"
    "ds_read_b64_tr_b16 a[72:73], v112 offset:2048\n"
    "v_exp_f32_e32 v86, v86\n"
    "ds_read_b64_tr_b16 a[74:75], v112 offset:2560\n"
    "v_exp_f32_e32 v87, v87\n"
    "ds_read_b64_tr_b16 a[76:77], v112 offset:3072\n"
    "v_exp_f32_e64 v88, v88\n"
    "ds_read_u16_d16_hi v49, v49 offset:65472\n"
    "v_mfma_f32_16x16x16bf16_1k v[96:99], a[100:101], a[118:119], 0\n"
    "ds_read_b64_tr_b16 a[78:79], v112 offset:3584\n"
    "v_exp_f32_e32 v89, v89\n"
    "v_mfma_f32_32x32x8bf16_1k v[16:31], a[100:101], a[110:111], 0\n"
    "v_exp_f32_e32 v90, v90\n"
    "ds_read_u16 v48, v48 offset:65472\n"
    "v_exp_f32_e32 v91, v91\n"
    "v_exp_f32_e32 v92, v92\n"
    "v_exp_f32_e32 v93, v93\n"
    "s_nop 2\n"
    "v_mfma_f32_16x16x4bf16_1k v[80:95], a[122:123], a[122:123], v[80:95]\n"
    "v_lshlrev_b32_sdwa v100, 1, v100 dst_sel:DWORD dst_unused:UNUSED_PAD src0_sel:DWORD src1_sel:WORD_1\n"
    "ds_read_u16 v100, v100 offset:65472\n"
    "s_waitcnt lgkmcnt(15)\n"
    "v_or_b32_e32 v63, v62, v63\n"
    "v_lshlrev_b32_sdwa v101, 1, v101 dst_sel:DWORD dst_unused:UNUSED_PAD src0_sel:DWORD src1_sel:WORD_1\n"
    "v_or_b32_e64 v62, v60, v61\n"
    "v_lshlrev_b32_sdwa v102, 1, v102 dst_sel:DWORD dst_unused:UNUSED_PAD src0_sel:DWORD src1_sel:WORD_1\n"
    "s_add_u32 m0, s47, 2048\n"
    "v_or_b32_e64 v61, v58, v59\n"
    "v_lshlrev_b32_sdwa v103, 1, v103 dst_sel:DWORD dst_unused:UNUSED_PAD src0_sel:DWORD src1_sel:WORD_1\n"
    "ds_read_u16_d16_hi v101, v101 offset:65472\n"
    "s_waitcnt lgkmcnt(14)\n"
    "v_or_b32_e32 v60, v56, v57\n"
    "v_lshlrev_b32_sdwa v46, 1, v46 dst_sel:DWORD dst_unused:UNUSED_PAD src0_sel:DWORD src1_sel:WORD_1\n"
    "s_waitcnt lgkmcnt(3)\n"
    "buffer_load_dwordx4 v120, s[28:31], s49 offen lds\n"
    "v_or_b32_e32 v59, v54, v55\n"
    "v_lshlrev_b32_sdwa v47, 1, v47 dst_sel:DWORD dst_unused:UNUSED_PAD src0_sel:DWORD src1_sel:WORD_1\n"
    "v_or_b32_e32 v58, v52, v53\n"
    "v_or_b32_e32 v57, v50, v51\n"
    "ds_read_u16 v102, v102 offset:65472\n"
    "buffer_load_dwordx4 v120, s[28:31], s49 offen offset:1024 lds\n"
    "s_waitcnt lgkmcnt(3)\n"
    "v_or_b32_e32 v56, v48, v49\n"
    "v_mfma_f32_32x32x16_bf16 a[16:31], v[60:63], a[84:87], a[16:31]\n"
    "s_add_u32 s49, s49, 2048\n"
    "v_lshlrev_b32_sdwa v79, 1, v79 dst_sel:DWORD dst_unused:UNUSED_PAD src0_sel:DWORD src1_sel:WORD_1\n"
    "v_lshlrev_b32_sdwa v78, 1, v78 dst_sel:DWORD dst_unused:UNUSED_PAD src0_sel:DWORD src1_sel:WORD_1\n"
    "v_lshlrev_b32_sdwa v77, 1, v77 dst_sel:DWORD dst_unused:UNUSED_PAD src0_sel:DWORD src1_sel:WORD_1\n"
    "v_mfma_f32_32x32x16_bf16 a[16:31], v[56:59], a[80:83], a[16:31]\n"
    "ds_read_u16_d16_hi v103, v103 offset:65472\n"
    "v_lshlrev_b32_sdwa v76, 1, v76 dst_sel:DWORD dst_unused:UNUSED_PAD src0_sel:DWORD src1_sel:WORD_1\n"
    "v_lshlrev_b32_sdwa v75, 1, v75 dst_sel:DWORD dst_unused:UNUSED_PAD src0_sel:DWORD src1_sel:WORD_1\n"
    "v_lshlrev_b32_sdwa v74, 1, v74 dst_sel:DWORD dst_unused:UNUSED_PAD src0_sel:DWORD src1_sel:WORD_1\n"
    "v_lshlrev_b32_sdwa v73, 1, v73 dst_sel:DWORD dst_unused:UNUSED_PAD src0_sel:DWORD src1_sel:WORD_1\n"
    "v_mfma_f32_32x32x16_bf16 v[48:63], a[72:75], a[56:59], v[16:31]\n"
    "ds_read_u16 v46, v46 offset:65472\n"
    "v_lshlrev_b32_sdwa v72, 1, v72 dst_sel:DWORD dst_unused:UNUSED_PAD src0_sel:DWORD src1_sel:WORD_1\n"
    "v_lshlrev_b32_sdwa v71, 1, v71 dst_sel:DWORD dst_unused:UNUSED_PAD src0_sel:DWORD src1_sel:WORD_1\n"
    "v_lshlrev_b32_sdwa v70, 1, v70 dst_sel:DWORD dst_unused:UNUSED_PAD src0_sel:DWORD src1_sel:WORD_1\n"
    "s_waitcnt vmcnt(4)\n"
    "ds_read_b64_tr_b16 a[80:81], v112 offset:4096\n"
    "v_lshlrev_b32_sdwa v69, 1, v69 dst_sel:DWORD dst_unused:UNUSED_PAD src0_sel:DWORD src1_sel:WORD_1\n"
    "v_mfma_f32_32x32x16_bf16 v[16:31], a[72:75], a[48:51], v[16:31]\n"
    "ds_read_b64_tr_b16 a[82:83], v112 offset:4608\n"
    "v_lshlrev_b32_sdwa v68, 1, v68 dst_sel:DWORD dst_unused:UNUSED_PAD src0_sel:DWORD src1_sel:WORD_1\n"
    "ds_read_u16_d16_hi v47, v47 offset:65472\n"
    "ds_read_b64_tr_b16 a[84:85], v112 offset:5120\n"
    "v_lshlrev_b32_sdwa v67, 1, v67 dst_sel:DWORD dst_unused:UNUSED_PAD src0_sel:DWORD src1_sel:WORD_1\n"
    "ds_read_b64_tr_b16 a[86:87], v112 offset:5632\n"
    "v_lshlrev_b32_sdwa v66, 1, v66 dst_sel:DWORD dst_unused:UNUSED_PAD src0_sel:DWORD src1_sel:WORD_1\n"
    "ds_read_b64_tr_b16 a[90:91], v113 offset:4096\n"
    "v_lshlrev_b32_sdwa v65, 1, v65 dst_sel:DWORD dst_unused:UNUSED_PAD src0_sel:DWORD src1_sel:WORD_1\n"
    "v_mfma_f32_32x32x16_bf16 v[16:31], a[76:79], a[52:55], v[16:31]\n"
    "ds_read_b64_tr_b16 a[94:95], v113 offset:4128\n"
    "v_lshlrev_b32_sdwa v64, 1, v64 dst_sel:DWORD dst_unused:UNUSED_PAD src0_sel:DWORD src1_sel:WORD_1\n"
    "ds_read_b64_tr_b16 a[126:127], v113 offset:5120\n"
    "ds_read_b64_tr_b16 v[6:7], v113 offset:5152\n"
    "v_rcp_f32_e32 v80, v80\n"
    "ds_read_u16_d16_hi v79, v79 offset:65472\n"
    "v_rcp_f32_e32 v81, v81\n"
    "v_mfma_f32_32x32x16_bf16 v[48:63], a[76:79], a[60:63], v[48:63]\n"
    "v_rcp_f32_e32 v82, v82\n"
    "ds_read_u16 v78, v78 offset:65472\n"
    "v_rcp_f32_e32 v83, v83\n"
    "v_rcp_f32_e32 v84, v84\n"
    "v_mfma_f32_16x16x32_bf16 v[96:99], a[72:75], a[64:67], v[96:99]\n"
    "ds_read_u16_d16_hi v77, v77 offset:65472\n"
    "v_rcp_f32_e64 v85, v85\n"
    "v_mfma_f32_16x16x32_bf16 v[96:99], a[76:79], a[68:71], v[96:99]\n"
    "v_rcp_f32_e32 v86, v86\n"
    "v_rcp_f32_e32 v87, v87\n"
    "ds_read_u16 v76, v76 offset:65472\n"
    "v_rcp_f32_e32 v88, v88\n"
    "v_rcp_f32_e32 v89, v89\n"
    "v_rcp_f32_e64 v90, v90\n"
    "ds_read_u16_d16_hi v75, v75 offset:65472\n"
    "v_rcp_f32_e32 v91, v91\n"
    "v_rcp_f32_e32 v92, v92\n"
    "v_rcp_f32_e32 v93, v93\n"
    "ds_read_u16 v74, v74 offset:65472\n"
    "s_cmp_lt_u32 s44, 4\n"
    "s_cbranch_scc0 Lq10%=\n"
    "s_setprio 0\n"
    "Lq10%=:\n"
    "v_pk_mul_f32 v[32:33], v[32:33], v[80:81]\n"
    "v_pk_mul_f32 v[34:35], v[34:35], v[82:83]\n"
    "v_pk_mul_f32 v[36:37], v[36:37], v[84:85]\n"
    "v_pk_mul_f32 v[38:39], v[38:39], v[86:87]\n"
    "ds_read_u16_d16_hi v73, v73 offset:65472\n"
    "s_add_u32 m0, s47, 4096\n"
    "v_pk_mul_f32 v[40:41], v[40:41], v[88:89]\n"
    "v_pk_mul_f32 v[42:43], v[42:43], v[90:91]\n"
    "s_waitcnt lgkmcnt(7)\n"
    "buffer_load_dwordx4 v116, s[24:27], s48 offen lds\n"
    "v_pk_mul_f32 v[44:45], v[44:45], v[92:93]\n"
    "buffer_load_dwordx4 v117, s[24:27], s48 offen offset:1024 lds\n"
    "s_add_u32 s48, s48, 64\n"
    "v_pk_fma_f32 v[80:81], v[16:17], v[16:17], s[50:51] neg_lo:[0,1,0] neg_hi:[0,1,0]\n"
    "v_pk_fma_f32 v[82:83], v[18:19], v[18:19], s[50:51] neg_lo:[0,1,0] neg_hi:[0,1,0]\n"
    "v_pk_fma_f32 v[84:85], v[20:21], v[20:21], s[50:51] neg_lo:[0,1,0] neg_hi:[0,1,0]\n"
    "ds_read_u16 v72, v72 offset:65472\n"
    "v_pk_fma_f32 v[86:87], v[22:23], v[22:23], s[50:51] neg_lo:[0,1,0] neg_hi:[0,1,0]\n"
    "v_pk_fma_f32 v[88:89], v[24:25], v[24:25], s[50:51] neg_lo:[0,1,0] neg_hi:[0,1,0]\n"
    "v_pk_fma_f32 v[90:91], v[26:27], v[26:27], s[50:51] neg_lo:[0,1,0] neg_hi:[0,1,0]\n"
    "v_pk_fma_f32 v[92:93], v[28:29], v[28:29], s[50:51] neg_lo:[0,1,0] neg_hi:[0,1,0]\n"
    "v_pk_mul_f32 v[80:81], v[80:81], v[16:17]\n"
    "v_pk_mul_f32 v[82:83], v[82:83], v[18:19]\n"
    "ds_read_u16_d16_hi v71, v71 offset:65472\n"
    "v_pk_mul_f32 v[84:85], v[84:85], v[20:21]\n"
    "v_pk_mul_f32 v[86:87], v[86:87], v[22:23]\n"
    "v_pk_mul_f32 v[88:89], v[88:89], v[24:25]\n"
    "v_pk_mul_f32 v[90:91], v[90:91], v[26:27]\n"
    "v_pk_mul_f32 v[92:93], v[92:93], v[28:29]\n"
    "v_cvt_pk_bf16_f32 v32, v32, v33\n"
    "ds_read_u16 v70, v70 offset:65472\n"
    "v_cvt_pk_bf16_f32 v33, v34, v35\n"
    "v_cvt_pk_bf16_f32 v34, v36, v37\n"
    "v_cvt_pk_bf16_f32 v35, v38, v39\n"
    "v_cvt_pk_bf16_f32 v36, v40, v41\n"
    "v_cvt_pk_bf16_f32 v37, v42, v43\n"
    "ds_read_u16_d16_hi v69, v69 offset:65472\n"
    "v_cvt_pk_bf16_f32 v38, v44, v45\n"
    "v_or_b32_e32 v39, v46, v47\n"
    "v_or_b32_e32 v110, v100, v101\n"
    "v_or_b32_e32 v111, v102, v103\n"
    "v_mfma_f32_32x32x16_bf16 a[0:15], v[32:35], a[80:83], a[0:15]\n"
    "v_exp_f32_e32 v80, v80\n"
    "ds_read_u16 v68, v68 offset:65472\n"
    "v_exp_f32_e32 v81, v81\n"
    "v_mfma_f32_32x32x16_bf16 a[0:15], v[36:39], a[84:87], a[0:15]\n"
    "v_exp_f32_e32 v82, v82\n"
    "ds_read_u16_d16_hi v67, v67 offset:65472\n"
    "v_exp_f32_e64 v83, v83\n"
    "v_mfma_f32_16x16x32_bf16 a[32:35], v[108:111], a[88:91], a[32:35]\n"
    "v_exp_f32_e32 v84, v84\n"
    "v_exp_f32_e32 v85, v85\n"
    "ds_read_u16 v66, v66 offset:65472\n"
    "v_mfma_f32_16x16x32_bf16 a[36:39], v[108:111], a[92:95], a[36:39]\n"
    "s_waitcnt vmcnt(4)\n"
    "ds_read_b64_tr_b16 a[72:73], v112\n"
    "v_exp_f32_e32 v86, v86\n"
    "v_mfma_f32_16x16x32_bf16 a[40:43], v[108:111], a[124:127], a[40:43]\n"
    "ds_read_u16_d16_hi v65, v65 offset:65472\n"
    "ds_read_b64_tr_b16 a[74:75], v112 offset:512\n"
    "v_exp_f32_e64 v87, v87\n"
    "v_mfma_f32_16x16x32_bf16 a[44:47], v[108:111], v[4:7], a[44:47]\n"
    "ds_read_b64_tr_b16 a[76:77], v112 offset:1024\n"
    "v_exp_f32_e64 v88, v88\n"
    "v_mfma_f32_16x16x16bf16_1k v[100:103], a[100:101], a[120:121], 0\n"
    "ds_read_u16 v64, v64 offset:65472\n"
    "ds_read_b64_tr_b16 a[78:79], v112 offset:1536\n"
    "v_exp_f32_e32 v89, v89\n"
    "v_mfma_f32_32x32x8bf16_1k v[32:47], a[100:101], a[112:113], 0\n"
    "v_exp_f32_e32 v90, v90\n"
    "v_exp_f32_e32 v91, v91\n"
    "v_exp_f32_e32 v92, v92\n"
    "v_exp_f32_e32 v93, v93\n"
    "s_nop 2\n"
    "v_mfma_f32_16x16x4bf16_1k v[80:95], a[122:123], a[122:123], v[80:95]\n"
    "v_lshlrev_b32_sdwa v96, 1, v96 dst_sel:DWORD dst_unused:UNUSED_PAD src0_sel:DWORD src1_sel:WORD_1\n"
    "ds_read_u16 v96, v96 offset:65472\n"
    "s_waitcnt lgkmcnt(15)\n"
    "v_or_b32_e32 v79, v78, v79\n"
    "v_lshlrev_b32_sdwa v97, 1, v97 dst_sel:DWORD dst_unused:UNUSED_PAD src0_sel:DWORD src1_sel:WORD_1\n"
    "v_or_b32_e64 v78, v76, v77\n"
    "v_lshlrev_b32_sdwa v98, 1, v98 dst_sel:DWORD dst_unused:UNUSED_PAD src0_sel:DWORD src1_sel:WORD_1\n"
    "s_mov_b32 m0, s47\n"
    "v_or_b32_e32 v77, v74, v75\n"
    "v_lshlrev_b32_sdwa v99, 1, v99 dst_sel:DWORD dst_unused:UNUSED_PAD src0_sel:DWORD src1_sel:WORD_1\n"
    "ds_read_u16_d16_hi v97, v97 offset:65472\n"
    "s_waitcnt lgkmcnt(14)\n"
    "v_or_b32_e32 v76, v72, v73\n"
    "v_lshlrev_b32_sdwa v30, 1, v30 dst_sel:DWORD dst_unused:UNUSED_PAD src0_sel:DWORD src1_sel:WORD_1\n"
    "s_waitcnt lgkmcnt(2)\n"
    "buffer_load_dwordx4 v120, s[28:31], s49 offen lds\n"
    "v_or_b32_e32 v75, v70, v71\n"
    "v_lshlrev_b32_sdwa v31, 1, v31 dst_sel:DWORD dst_unused:UNUSED_PAD src0_sel:DWORD src1_sel:WORD_1\n"
    "v_or_b32_e32 v74, v68, v69\n"
    "v_or_b32_e32 v73, v66, v67\n"
    "ds_read_u16 v98, v98 offset:65472\n"
    "buffer_load_dwordx4 v120, s[28:31], s49 offen offset:1024 lds\n"
    "v_or_b32_e32 v72, v64, v65\n"
    "v_mfma_f32_32x32x16_bf16 a[16:31], v[76:79], a[84:87], a[16:31]\n"
    "s_add_u32 s49, s49, 2048\n"
    "v_lshlrev_b32_sdwa v63, 1, v63 dst_sel:DWORD dst_unused:UNUSED_PAD src0_sel:DWORD src1_sel:WORD_1\n"
    "v_lshlrev_b32_sdwa v62, 1, v62 dst_sel:DWORD dst_unused:UNUSED_PAD src0_sel:DWORD src1_sel:WORD_1\n"
    "v_lshlrev_b32_sdwa v61, 1, v61 dst_sel:DWORD dst_unused:UNUSED_PAD src0_sel:DWORD src1_sel:WORD_1\n"
    "v_mfma_f32_32x32x16_bf16 a[16:31], v[72:75], a[80:83], a[16:31]\n"
    "ds_read_u16_d16_hi v99, v99 offset:65472\n"
    "v_lshlrev_b32_sdwa v60, 1, v60 dst_sel:DWORD dst_unused:UNUSED_PAD src0_sel:DWORD src1_sel:WORD_1\n"
    "v_lshlrev_b32_sdwa v59, 1, v59 dst_sel:DWORD dst_unused:UNUSED_PAD src0_sel:DWORD src1_sel:WORD_1\n"
    "v_lshlrev_b32_sdwa v58, 1, v58 dst_sel:DWORD dst_unused:UNUSED_PAD src0_sel:DWORD src1_sel:WORD_1\n"
    "v_lshlrev_b32_sdwa v57, 1, v57 dst_sel:DWORD dst_unused:UNUSED_PAD src0_sel:DWORD src1_sel:WORD_1\n"
    "v_mfma_f32_32x32x16_bf16 v[64:79], a[72:75], a[56:59], v[32:47]\n"
    "ds_read_u16 v30, v30 offset:65472\n"
    "v_lshlrev_b32_sdwa v56, 1, v56 dst_sel:DWORD dst_unused:UNUSED_PAD src0_sel:DWORD src1_sel:WORD_1\n"
    "v_lshlrev_b32_sdwa v55, 1, v55 dst_sel:DWORD dst_unused:UNUSED_PAD src0_sel:DWORD src1_sel:WORD_1\n"
    "v_lshlrev_b32_sdwa v54, 1, v54 dst_sel:DWORD dst_unused:UNUSED_PAD src0_sel:DWORD src1_sel:WORD_1\n"
    "s_waitcnt vmcnt(4)\n"
    "ds_read_b64_tr_b16 a[80:81], v112 offset:2048\n"
    "v_lshlrev_b32_sdwa v53, 1, v53 dst_sel:DWORD dst_unused:UNUSED_PAD src0_sel:DWORD src1_sel:WORD_1\n"
    "v_mfma_f32_32x32x16_bf16 v[32:47], a[72:75], a[48:51], v[32:47]\n"
    "ds_read_b64_tr_b16 a[82:83], v112 offset:2560\n"
    "v_lshlrev_b32_sdwa v52, 1, v52 dst_sel:DWORD dst_unused:UNUSED_PAD src0_sel:DWORD src1_sel:WORD_1\n"
    "ds_read_u16_d16_hi v31, v31 offset:65472\n"
    "ds_read_b64_tr_b16 a[84:85], v112 offset:3072\n"
    "v_lshlrev_b32_sdwa v51, 1, v51 dst_sel:DWORD dst_unused:UNUSED_PAD src0_sel:DWORD src1_sel:WORD_1\n"
    "ds_read_b64_tr_b16 a[86:87], v112 offset:3584\n"
    "v_lshlrev_b32_sdwa v50, 1, v50 dst_sel:DWORD dst_unused:UNUSED_PAD src0_sel:DWORD src1_sel:WORD_1\n"
    "ds_read_b64_tr_b16 a[88:89], v113 offset:2048\n"
    "v_lshlrev_b32_sdwa v49, 1, v49 dst_sel:DWORD dst_unused:UNUSED_PAD src0_sel:DWORD src1_sel:WORD_1\n"
    "v_mfma_f32_32x32x16_bf16 v[32:47], a[76:79], a[52:55], v[32:47]\n"
    "ds_read_b64_tr_b16 a[92:93], v113 offset:2080\n"
    "v_lshlrev_b32_sdwa v48, 1, v48 dst_sel:DWORD dst_unused:UNUSED_PAD src0_sel:DWORD src1_sel:WORD_1\n"
    "ds_read_b64_tr_b16 a[124:125], v113 offset:3072\n"
    "ds_read_b64_tr_b16 v[4:5], v113 offset:3104\n"
    "v_rcp_f32_e32 v80, v80\n"
    "ds_read_u16_d16_hi v63, v63 offset:65472\n"
    "v_rcp_f32_e32 v81, v81\n"
    "v_mfma_f32_32x32x16_bf16 v[64:79], a[76:79], a[60:63], v[64:79]\n"
    "v_rcp_f32_e64 v82, v82\n"
    "ds_read_u16 v62, v62 offset:65472\n"
    "v_rcp_f32_e32 v83, v83\n"
    "v_rcp_f32_e32 v84, v84\n"
    "v_mfma_f32_16x16x32_bf16 v[100:103], a[72:75], a[64:67], v[100:103]\n"
    "ds_read_u16_d16_hi v61, v61 offset:65472\n"
    "v_rcp_f32_e64 v85, v85\n"
    "v_mfma_f32_16x16x32_bf16 v[100:103], a[76:79], a[68:71], v[100:103]\n"
    "v_rcp_f32_e32 v86, v86\n"
    "v_rcp_f32_e32 v87, v87\n"
    "ds_read_u16 v60, v60 offset:65472\n"
    "v_rcp_f32_e32 v88, v88\n"
    "v_rcp_f32_e32 v89, v89\n"
    "v_rcp_f32_e32 v90, v90\n"
    "ds_read_u16_d16_hi v59, v59 offset:65472\n"
    "v_rcp_f32_e32 v91, v91\n"
    "v_rcp_f32_e32 v92, v92\n"
    "v_rcp_f32_e32 v93, v93\n"
    "ds_read_u16 v58, v58 offset:65472\n"
    "v_pk_mul_f32 v[16:17], v[16:17], v[80:81]\n"
    "v_pk_mul_f32 v[18:19], v[18:19], v[82:83]\n"
    "v_pk_mul_f32 v[20:21], v[20:21], v[84:85]\n"
    "v_pk_mul_f32 v[22:23], v[22:23], v[86:87]\n"
    "ds_read_u16_d16_hi v57, v57 offset:65472\n"
    "s_add_u32 m0, s47, 2048\n"
    "v_pk_mul_f32 v[24:25], v[24:25], v[88:89]\n"
    "v_pk_mul_f32 v[26:27], v[26:27], v[90:91]\n"
    "s_waitcnt lgkmcnt(7)\n"
    "buffer_load_dwordx4 v116, s[24:27], s48 offen lds\n"
    "v_pk_mul_f32 v[28:29], v[28:29], v[92:93]\n"
    "buffer_load_dwordx4 v117, s[24:27], s48 offen offset:1024 lds\n"
    "s_add_u32 s48, s48, 64\n"
    "v_pk_fma_f32 v[80:81], v[32:33], v[32:33], s[50:51] neg_lo:[0,1,0] neg_hi:[0,1,0]\n"
    "v_pk_fma_f32 v[82:83], v[34:35], v[34:35], s[50:51] neg_lo:[0,1,0] neg_hi:[0,1,0]\n"
    "v_pk_fma_f32 v[84:85], v[36:37], v[36:37], s[50:51] neg_lo:[0,1,0] neg_hi:[0,1,0]\n"
    "ds_read_u16 v56, v56 offset:65472\n"
    "v_pk_fma_f32 v[86:87], v[38:39], v[38:39], s[50:51] neg_lo:[0,1,0] neg_hi:[0,1,0]\n"
    "v_pk_fma_f32 v[88:89], v[40:41], v[40:41], s[50:51] neg_lo:[0,1,0] neg_hi:[0,1,0]\n"
    "v_pk_fma_f32 v[90:91], v[42:43], v[42:43], s[50:51] neg_lo:[0,1,0] neg_hi:[0,1,0]\n"
    "v_pk_fma_f32 v[92:93], v[44:45], v[44:45], s[50:51] neg_lo:[0,1,0] neg_hi:[0,1,0]\n"
    "v_pk_mul_f32 v[80:81], v[80:81], v[32:33]\n"
    "v_pk_mul_f32 v[82:83], v[82:83], v[34:35]\n"
    "ds_read_u16_d16_hi v55, v55 offset:65472\n"
    "v_pk_mul_f32 v[84:85], v[84:85], v[36:37]\n"
    "v_pk_mul_f32 v[86:87], v[86:87], v[38:39]\n"
    "v_pk_mul_f32 v[88:89], v[88:89], v[40:41]\n"
    "v_pk_mul_f32 v[90:91], v[90:91], v[42:43]\n"
    "v_pk_mul_f32 v[92:93], v[92:93], v[44:45]\n"
    "v_cvt_pk_bf16_f32 v16, v16, v17\n"
    "ds_read_u16 v54, v54 offset:65472\n"
    "v_cvt_pk_bf16_f32 v17, v18, v19\n"
    "v_cvt_pk_bf16_f32 v18, v20, v21\n"
    "v_cvt_pk_bf16_f32 v19, v22, v23\n"
    "v_cvt_pk_bf16_f32 v20, v24, v25\n"
    "v_cvt_pk_bf16_f32 v21, v26, v27\n"
    "ds_read_u16_d16_hi v53, v53 offset:65472\n"
    "v_cvt_pk_bf16_f32 v22, v28, v29\n"
    "v_or_b32_e32 v23, v30, v31\n"
    "v_or_b32_e32 v108, v96, v97\n"
    "v_or_b32_e32 v109, v98, v99\n"
    "v_mfma_f32_32x32x16_bf16 a[0:15], v[16:19], a[80:83], a[0:15]\n"
    "v_exp_f32_e32 v80, v80\n"
    "ds_read_u16 v52, v52 offset:65472\n"
    "v_exp_f32_e32 v81, v81\n"
    "v_mfma_f32_32x32x16_bf16 a[0:15], v[20:23], a[84:87], a[0:15]\n"
    "v_exp_f32_e32 v82, v82\n"
    "ds_read_u16_d16_hi v51, v51 offset:65472\n"
    "v_exp_f32_e32 v83, v83\n"
    "v_exp_f32_e32 v84, v84\n"
    "v_exp_f32_e32 v85, v85\n"
    "ds_read_u16 v50, v50 offset:65472\n"
    "s_waitcnt vmcnt(4)\n"
    "ds_read_b64_tr_b16 a[72:73], v112 offset:4096\n"
    "v_exp_f32_e32 v86, v86\n"
    "ds_read_b64_tr_b16 a[74:75], v112 offset:4608\n"
    "v_exp_f32_e32 v87, v87\n"
    "ds_read_b64_tr_b16 a[76:77], v112 offset:5120\n"
    "v_exp_f32_e64 v88, v88\n"
    "ds_read_u16_d16_hi v49, v49 offset:65472\n"
    "v_mfma_f32_16x16x16bf16_1k v[96:99], a[102:103], a[114:115], 0\n"
    "ds_read_b64_tr_b16 a[78:79], v112 offset:5632\n"
    "v_exp_f32_e32 v89, v89\n"
    "v_mfma_f32_32x32x8bf16_1k v[16:31], a[102:103], a[106:107], 0\n"
    "v_exp_f32_e32 v90, v90\n"
    "ds_read_u16 v48, v48 offset:65472\n"
    "v_exp_f32_e32 v91, v91\n"
    "v_exp_f32_e32 v92, v92\n"
    "v_exp_f32_e32 v93, v93\n"
    "s_nop 2\n"
    "v_mfma_f32_16x16x4bf16_1k v[80:95], a[122:123], a[122:123], v[80:95]\n"
    "v_lshlrev_b32_sdwa v100, 1, v100 dst_sel:DWORD dst_unused:UNUSED_PAD src0_sel:DWORD src1_sel:WORD_1\n"
    "ds_read_u16 v100, v100 offset:65472\n"
    "s_waitcnt lgkmcnt(15)\n"
    "v_or_b32_e32 v63, v62, v63\n"
    "v_lshlrev_b32_sdwa v101, 1, v101 dst_sel:DWORD dst_unused:UNUSED_PAD src0_sel:DWORD src1_sel:WORD_1\n"
    "v_or_b32_e64 v62, v60, v61\n"
    "v_lshlrev_b32_sdwa v102, 1, v102 dst_sel:DWORD dst_unused:UNUSED_PAD src0_sel:DWORD src1_sel:WORD_1\n"
    "s_add_u32 m0, s47, 4096\n"
    "v_or_b32_e64 v61, v58, v59\n"
    "v_lshlrev_b32_sdwa v103, 1, v103 dst_sel:DWORD dst_unused:UNUSED_PAD src0_sel:DWORD src1_sel:WORD_1\n"
    "ds_read_u16_d16_hi v101, v101 offset:65472\n"
    "s_waitcnt lgkmcnt(14)\n"
    "v_or_b32_e32 v60, v56, v57\n"
    "v_lshlrev_b32_sdwa v46, 1, v46 dst_sel:DWORD dst_unused:UNUSED_PAD src0_sel:DWORD src1_sel:WORD_1\n"
    "s_waitcnt lgkmcnt(3)\n"
    "buffer_load_dwordx4 v120, s[28:31], s49 offen lds\n"
    "v_or_b32_e32 v59, v54, v55\n"
    "v_lshlrev_b32_sdwa v47, 1, v47 dst_sel:DWORD dst_unused:UNUSED_PAD src0_sel:DWORD src1_sel:WORD_1\n"
    "v_or_b32_e32 v58, v52, v53\n"
    "v_or_b32_e32 v57, v50, v51\n"
    "ds_read_u16 v102, v102 offset:65472\n"
    "buffer_load_dwordx4 v120, s[28:31], s49 offen offset:1024 lds\n"
    "s_waitcnt lgkmcnt(3)\n"
    "v_or_b32_e32 v56, v48, v49\n"
    "v_mfma_f32_32x32x16_bf16 a[16:31], v[60:63], a[84:87], a[16:31]\n"
    "s_add_u32 s49, s49, 2048\n"
    "v_lshlrev_b32_sdwa v79, 1, v79 dst_sel:DWORD dst_unused:UNUSED_PAD src0_sel:DWORD src1_sel:WORD_1\n"
    "v_lshlrev_b32_sdwa v78, 1, v78 dst_sel:DWORD dst_unused:UNUSED_PAD src0_sel:DWORD src1_sel:WORD_1\n"
    "v_lshlrev_b32_sdwa v77, 1, v77 dst_sel:DWORD dst_unused:UNUSED_PAD src0_sel:DWORD src1_sel:WORD_1\n"
    "v_mfma_f32_32x32x16_bf16 a[16:31], v[56:59], a[80:83], a[16:31]\n"
    "ds_read_u16_d16_hi v103, v103 offset:65472\n"
    "v_lshlrev_b32_sdwa v76, 1, v76 dst_sel:DWORD dst_unused:UNUSED_PAD src0_sel:DWORD src1_sel:WORD_1\n"
    "v_lshlrev_b32_sdwa v75, 1, v75 dst_sel:DWORD dst_unused:UNUSED_PAD src0_sel:DWORD src1_sel:WORD_1\n"
    "v_lshlrev_b32_sdwa v74, 1, v74 dst_sel:DWORD dst_unused:UNUSED_PAD src0_sel:DWORD src1_sel:WORD_1\n"
    "v_lshlrev_b32_sdwa v73, 1, v73 dst_sel:DWORD dst_unused:UNUSED_PAD src0_sel:DWORD src1_sel:WORD_1\n"
    "v_mfma_f32_32x32x16_bf16 v[48:63], a[72:75], a[56:59], v[16:31]\n"
    "ds_read_u16 v46, v46 offset:65472\n"
    "v_lshlrev_b32_sdwa v72, 1, v72 dst_sel:DWORD dst_unused:UNUSED_PAD src0_sel:DWORD src1_sel:WORD_1\n"
    "v_lshlrev_b32_sdwa v71, 1, v71 dst_sel:DWORD dst_unused:UNUSED_PAD src0_sel:DWORD src1_sel:WORD_1\n"
    "v_lshlrev_b32_sdwa v70, 1, v70 dst_sel:DWORD dst_unused:UNUSED_PAD src0_sel:DWORD src1_sel:WORD_1\n"
    "s_waitcnt vmcnt(4)\n"
    "ds_read_b64_tr_b16 a[80:81], v112\n"
    "v_lshlrev_b32_sdwa v69, 1, v69 dst_sel:DWORD dst_unused:UNUSED_PAD src0_sel:DWORD src1_sel:WORD_1\n"
    "v_mfma_f32_32x32x16_bf16 v[16:31], a[72:75], a[48:51], v[16:31]\n"
    "ds_read_b64_tr_b16 a[82:83], v112 offset:512\n"
    "v_lshlrev_b32_sdwa v68, 1, v68 dst_sel:DWORD dst_unused:UNUSED_PAD src0_sel:DWORD src1_sel:WORD_1\n"
    "ds_read_u16_d16_hi v47, v47 offset:65472\n"
    "ds_read_b64_tr_b16 a[84:85], v112 offset:1024\n"
    "v_lshlrev_b32_sdwa v67, 1, v67 dst_sel:DWORD dst_unused:UNUSED_PAD src0_sel:DWORD src1_sel:WORD_1\n"
    "ds_read_b64_tr_b16 a[86:87], v112 offset:1536\n"
    "v_lshlrev_b32_sdwa v66, 1, v66 dst_sel:DWORD dst_unused:UNUSED_PAD src0_sel:DWORD src1_sel:WORD_1\n"
    "ds_read_b64_tr_b16 a[90:91], v113\n"
    "v_lshlrev_b32_sdwa v65, 1, v65 dst_sel:DWORD dst_unused:UNUSED_PAD src0_sel:DWORD src1_sel:WORD_1\n"
    "v_mfma_f32_32x32x16_bf16 v[16:31], a[76:79], a[52:55], v[16:31]\n"
    "ds_read_b64_tr_b16 a[94:95], v113 offset:32\n"
    "v_lshlrev_b32_sdwa v64, 1, v64 dst_sel:DWORD dst_unused:UNUSED_PAD src0_sel:DWORD src1_sel:WORD_1\n"
    "ds_read_b64_tr_b16 a[126:127], v113 offset:1024\n"
    "ds_read_b64_tr_b16 v[6:7], v113 offset:1056\n"
    "v_rcp_f32_e32 v80, v80\n"
    "ds_read_u16_d16_hi v79, v79 offset:65472\n"
    "v_rcp_f32_e32 v81, v81\n"
    "v_mfma_f32_32x32x16_bf16 v[48:63], a[76:79], a[60:63], v[48:63]\n"
    "v_rcp_f32_e32 v82, v82\n"
    "ds_read_u16 v78, v78 offset:65472\n"
    "v_rcp_f32_e32 v83, v83\n"
    "v_rcp_f32_e32 v84, v84\n"
    "v_mfma_f32_16x16x32_bf16 v[96:99], a[72:75], a[64:67], v[96:99]\n"
    "ds_read_u16_d16_hi v77, v77 offset:65472\n"
    "v_rcp_f32_e64 v85, v85\n"
    "v_mfma_f32_16x16x32_bf16 v[96:99], a[76:79], a[68:71], v[96:99]\n"
    "v_rcp_f32_e32 v86, v86\n"
    "v_rcp_f32_e32 v87, v87\n"
    "ds_read_u16 v76, v76 offset:65472\n"
    "v_rcp_f32_e32 v88, v88\n"
    "v_rcp_f32_e32 v89, v89\n"
    "v_rcp_f32_e32 v90, v90\n"
    "ds_read_u16_d16_hi v75, v75 offset:65472\n"
    "v_rcp_f32_e32 v91, v91\n"
    "v_rcp_f32_e32 v92, v92\n"
    "v_rcp_f32_e32 v93, v93\n"
    "ds_read_u16 v74, v74 offset:65472\n"
    "s_cmp_lt_u32 s44, 4\n"
    "s_cbranch_scc0 Lq12%=\n"
    "s_setprio 2\n"
    "Lq12%=:\n"
    "v_pk_mul_f32 v[32:33], v[32:33], v[80:81]\n"
    "v_pk_mul_f32 v[34:35], v[34:35], v[82:83]\n"
    "v_pk_mul_f32 v[36:37], v[36:37], v[84:85]\n"
    "v_pk_mul_f32 v[38:39], v[38:39], v[86:87]\n"
    "ds_read_u16_d16_hi v73, v73 offset:65472\n"
    "s_mov_b32 m0, s47\n"
    "v_pk_mul_f32 v[40:41], v[40:41], v[88:89]\n"
    "v_pk_mul_f32 v[42:43], v[42:43], v[90:91]\n"
    "s_waitcnt lgkmcnt(7)\n"
    "buffer_load_dwordx4 v116, s[24:27], s48 offen lds\n"
    "v_pk_mul_f32 v[44:45], v[44:45], v[92:93]\n"
    "buffer_load_dwordx4 v117, s[24:27], s48 offen offset:1024 lds\n"
    "s_add_u32 s48, s48, 64\n"
    "v_pk_fma_f32 v[80:81], v[16:17], v[16:17], s[50:51] neg_lo:[0,1,0] neg_hi:[0,1,0]\n"
    "v_pk_fma_f32 v[82:83], v[18:19], v[18:19], s[50:51] neg_lo:[0,1,0] neg_hi:[0,1,0]\n"
    "v_pk_fma_f32 v[84:85], v[20:21], v[20:21], s[50:51] neg_lo:[0,1,0] neg_hi:[0,1,0]\n"
    "ds_read_u16 v72, v72 offset:65472\n"
    "v_pk_fma_f32 v[86:87], v[22:23], v[22:23], s[50:51] neg_lo:[0,1,0] neg_hi:[0,1,0]\n"
    "v_pk_fma_f32 v[88:89], v[24:25], v[24:25], s[50:51] neg_lo:[0,1,0] neg_hi:[0,1,0]\n"
    "v_pk_fma_f32 v[90:91], v[26:27], v[26:27], s[50:51] neg_lo:[0,1,0] neg_hi:[0,1,0]\n"
    "v_pk_fma_f32 v[92:93], v[28:29], v[28:29], s[50:51] neg_lo:[0,1,0] neg_hi:[0,1,0]\n"
    "v_pk_mul_f32 v[80:81], v[80:81], v[16:17]\n"
    "v_pk_mul_f32 v[82:83], v[82:83], v[18:19]\n"
    "ds_read_u16_d16_hi v71, v71 offset:65472\n"
    "v_pk_mul_f32 v[84:85], v[84:85], v[20:21]\n"
    "v_pk_mul_f32 v[86:87], v[86:87], v[22:23]\n"
    "v_pk_mul_f32 v[88:89], v[88:89], v[24:25]\n"
    "v_pk_mul_f32 v[90:91], v[90:91], v[26:27]\n"
    "v_pk_mul_f32 v[92:93], v[92:93], v[28:29]\n"
    "v_cvt_pk_bf16_f32 v32, v32, v33\n"
    "ds_read_u16 v70, v70 offset:65472\n"
    "v_cvt_pk_bf16_f32 v33, v34, v35\n"
    "v_cvt_pk_bf16_f32 v34, v36, v37\n"
    "v_cvt_pk_bf16_f32 v35, v38, v39\n"
    "v_cvt_pk_bf16_f32 v36, v40, v41\n"
    "v_cvt_pk_bf16_f32 v37, v42, v43\n"
    "ds_read_u16_d16_hi v69, v69 offset:65472\n"
    "v_cvt_pk_bf16_f32 v38, v44, v45\n"
    "v_or_b32_e32 v39, v46, v47\n"
    "v_or_b32_e32 v110, v100, v101\n"
    "v_or_b32_e32 v111, v102, v103\n"
    "v_mfma_f32_32x32x16_bf16 a[0:15], v[32:35], a[80:83], a[0:15]\n"
    "v_exp_f32_e32 v80, v80\n"
    "ds_read_u16 v68, v68 offset:65472\n"
    "v_exp_f32_e32 v81, v81\n"
    "v_mfma_f32_32x32x16_bf16 a[0:15], v[36:39], a[84:87], a[0:15]\n"
    "v_exp_f32_e32 v82, v82\n"
    "ds_read_u16_d16_hi v67, v67 offset:65472\n"
    "v_exp_f32_e64 v83, v83\n"
    "v_mfma_f32_16x16x32_bf16 a[32:35], v[108:111], a[88:91], a[32:35]\n"
    "v_exp_f32_e32 v84, v84\n"
    "v_exp_f32_e32 v85, v85\n"
    "ds_read_u16 v66, v66 offset:65472\n"
    "v_mfma_f32_16x16x32_bf16 a[36:39], v[108:111], a[92:95], a[36:39]\n"
    "s_waitcnt vmcnt(4)\n"
    "ds_read_b64_tr_b16 a[72:73], v112 offset:2048\n"
    "v_exp_f32_e32 v86, v86\n"
    "v_mfma_f32_16x16x32_bf16 a[40:43], v[108:111], a[124:127], a[40:43]\n"
    "ds_read_u16_d16_hi v65, v65 offset:65472\n"
    "ds_read_b64_tr_b16 a[74:75], v112 offset:2560\n"
    "v_exp_f32_e64 v87, v87\n"
    "v_mfma_f32_16x16x32_bf16 a[44:47], v[108:111], v[4:7], a[44:47]\n"
    "ds_read_b64_tr_b16 a[76:77], v112 offset:3072\n"
    "v_exp_f32_e64 v88, v88\n"
    "v_mfma_f32_16x16x16bf16_1k v[100:103], a[102:103], a[116:117], 0\n"
    "ds_read_u16 v64, v64 offset:65472\n"
    "ds_read_b64_tr_b16 a[78:79], v112 offset:3584\n"
    "v_exp_f32_e32 v89, v89\n"
    "v_mfma_f32_32x32x8bf16_1k v[32:47], a[102:103], a[108:109], 0\n"
    "v_exp_f32_e32 v90, v90\n"
    "v_exp_f32_e32 v91, v91\n"
    "v_exp_f32_e32 v92, v92\n"
    "v_exp_f32_e32 v93, v93\n"
    "s_nop 2\n"
    "v_mfma_f32_16x16x4bf16_1k v[80:95], a[122:123], a[122:123], v[80:95]\n"
    "v_lshlrev_b32_sdwa v96, 1, v96 dst_sel:DWORD dst_unused:UNUSED_PAD src0_sel:DWORD src1_sel:WORD_1\n"
    "ds_read_u16 v96, v96 offset:65472\n"
    "s_waitcnt lgkmcnt(15)\n"
    "v_or_b32_e32 v79, v78, v79\n"
    "v_lshlrev_b32_sdwa v97, 1, v97 dst_sel:DWORD dst_unused:UNUSED_PAD src0_sel:DWORD src1_sel:WORD_1\n"
    "v_or_b32_e64 v78, v76, v77\n"
    "v_lshlrev_b32_sdwa v98, 1, v98 dst_sel:DWORD dst_unused:UNUSED_PAD src0_sel:DWORD src1_sel:WORD_1\n"
    "s_add_u32 m0, s47, 2048\n"
    "v_or_b32_e64 v77, v74, v75\n"
    "v_lshlrev_b32_sdwa v99, 1, v99 dst_sel:DWORD dst_unused:UNUSED_PAD src0_sel:DWORD src1_sel:WORD_1\n"
    "ds_read_u16_d16_hi v97, v97 offset:65472\n"
    "s_waitcnt lgkmcnt(14)\n"
    "v_or_b32_e32 v76, v72, v73\n"
    "v_lshlrev_b32_sdwa v30, 1, v30 dst_sel:DWORD dst_unused:UNUSED_PAD src0_sel:DWORD src1_sel:WORD_1\n"
    "s_waitcnt lgkmcnt(2)\n"
    "buffer_load_dwordx4 v120, s[28:31], s49 offen lds\n"
    "v_or_b32_e32 v75, v70, v71\n"
    "v_lshlrev_b32_sdwa v31, 1, v31 dst_sel:DWORD dst_unused:UNUSED_PAD src0_sel:DWORD src1_sel:WORD_1\n"
    "v_or_b32_e32 v74, v68, v69\n"
    "v_or_b32_e32 v73, v66, v67\n"
    "ds_read_u16 v98, v98 offset:65472\n"
    "buffer_load_dwordx4 v120, s[28:31], s49 offen offset:1024 lds\n"
    "v_or_b32_e32 v72, v64, v65\n"
    "v_mfma_f32_32x32x16_bf16 a[16:31], v[76:79], a[84:87], a[16:31]\n"
    "s_add_u32 s49, s49, 2048\n"
    "v_lshlrev_b32_sdwa v63, 1, v63 dst_sel:DWORD dst_unused:UNUSED_PAD src0_sel:DWORD src1_sel:WORD_1\n"
    "v_lshlrev_b32_sdwa v62, 1, v62 dst_sel:DWORD dst_unused:UNUSED_PAD src0_sel:DWORD src1_sel:WORD_1\n"
    "v_lshlrev_b32_sdwa v61, 1, v61 dst_sel:DWORD dst_unused:UNUSED_PAD src0_sel:DWORD src1_sel:WORD_1\n"
    "v_mfma_f32_32x32x16_bf16 a[16:31], v[72:75], a[80:83], a[16:31]\n"
    "ds_read_u16_d16_hi v99, v99 offset:65472\n"
    "v_lshlrev_b32_sdwa v60, 1, v60 dst_sel:DWORD dst_unused:UNUSED_PAD src0_sel:DWORD src1_sel:WORD_1\n"
    "v_lshlrev_b32_sdwa v59, 1, v59 dst_sel:DWORD dst_unused:UNUSED_PAD src0_sel:DWORD src1_sel:WORD_1\n"
    "v_lshlrev_b32_sdwa v58, 1, v58 dst_sel:DWORD dst_unused:UNUSED_PAD src0_sel:DWORD src1_sel:WORD_1\n"
    "v_lshlrev_b32_sdwa v57, 1, v57 dst_sel:DWORD dst_unused:UNUSED_PAD src0_sel:DWORD src1_sel:WORD_1\n"
    "v_mfma_f32_32x32x16_bf16 v[64:79], a[72:75], a[56:59], v[32:47]\n"
    "ds_read_u16 v30, v30 offset:65472\n"
    "v_lshlrev_b32_sdwa v56, 1, v56 dst_sel:DWORD dst_unused:UNUSED_PAD src0_sel:DWORD src1_sel:WORD_1\n"
    "v_lshlrev_b32_sdwa v55, 1, v55 dst_sel:DWORD dst_unused:UNUSED_PAD src0_sel:DWORD src1_sel:WORD_1\n"
    "v_lshlrev_b32_sdwa v54, 1, v54 dst_sel:DWORD dst_unused:UNUSED_PAD src0_sel:DWORD src1_sel:WORD_1\n"
    "s_waitcnt vmcnt(4)\n"
    "ds_read_b64_tr_b16 a[80:81], v112 offset:4096\n"
    "v_lshlrev_b32_sdwa v53, 1, v53 dst_sel:DWORD dst_unused:UNUSED_PAD src0_sel:DWORD src1_sel:WORD_1\n"
    "v_mfma_f32_32x32x16_bf16 v[32:47], a[72:75], a[48:51], v[32:47]\n"
    "ds_read_b64_tr_b16 a[82:83], v112 offset:4608\n"
    "v_lshlrev_b32_sdwa v52, 1, v52 dst_sel:DWORD dst_unused:UNUSED_PAD src0_sel:DWORD src1_sel:WORD_1\n"
    "ds_read_u16_d16_hi v31, v31 offset:65472\n"
    "ds_read_b64_tr_b16 a[84:85], v112 offset:5120\n"
    "v_lshlrev_b32_sdwa v51, 1, v51 dst_sel:DWORD dst_unused:UNUSED_PAD src0_sel:DWORD src1_sel:WORD_1\n"
    "ds_read_b64_tr_b16 a[86:87], v112 offset:5632\n"
    "v_lshlrev_b32_sdwa v50, 1, v50 dst_sel:DWORD dst_unused:UNUSED_PAD src0_sel:DWORD src1_sel:WORD_1\n"
    "ds_read_b64_tr_b16 a[88:89], v113 offset:4096\n"
    "v_lshlrev_b32_sdwa v49, 1, v49 dst_sel:DWORD dst_unused:UNUSED_PAD src0_sel:DWORD src1_sel:WORD_1\n"
    "v_mfma_f32_32x32x16_bf16 v[32:47], a[76:79], a[52:55], v[32:47]\n"
    "ds_read_b64_tr_b16 a[92:93], v113 offset:4128\n"
    "v_lshlrev_b32_sdwa v48, 1, v48 dst_sel:DWORD dst_unused:UNUSED_PAD src0_sel:DWORD src1_sel:WORD_1\n"
    "ds_read_b64_tr_b16 a[124:125], v113 offset:5120\n"
    "ds_read_b64_tr_b16 v[4:5], v113 offset:5152\n"
    "v_rcp_f32_e32 v80, v80\n"
    "ds_read_u16_d16_hi v63, v63 offset:65472\n"
    "v_rcp_f32_e32 v81, v81\n"
    "v_mfma_f32_32x32x16_bf16 v[64:79], a[76:79], a[60:63], v[64:79]\n"
    "v_rcp_f32_e64 v82, v82\n"
    "ds_read_u16 v62, v62 offset:65472\n"
    "v_rcp_f32_e32 v83, v83\n"
    "v_rcp_f32_e32 v84, v84\n"
    "v_mfma_f32_16x16x32_bf16 v[100:103], a[72:75], a[64:67], v[100:103]\n"
    "ds_read_u16_d16_hi v61, v61 offset:65472\n"
    "v_rcp_f32_e64 v85, v85\n"
    "v_mfma_f32_16x16x32_bf16 v[100:103], a[76:79], a[68:71], v[100:103]\n"
    "v_rcp_f32_e32 v86, v86\n"
    "v_rcp_f32_e32 v87, v87\n"
    "ds_read_u16 v60, v60 offset:65472\n"
    "v_rcp_f32_e32 v88, v88\n"
    "v_rcp_f32_e32 v89, v89\n"
    "v_rcp_f32_e32 v90, v90\n"
    "ds_read_u16_d16_hi v59, v59 offset:65472\n"
    "v_rcp_f32_e32 v91, v91\n"
    "v_rcp_f32_e32 v92, v92\n"
    "v_rcp_f32_e32 v93, v93\n"
    "ds_read_u16 v58, v58 offset:65472\n"
    "v_pk_mul_f32 v[16:17], v[16:17], v[80:81]\n"
    "v_pk_mul_f32 v[18:19], v[18:19], v[82:83]\n"
    "v_pk_mul_f32 v[20:21], v[20:21], v[84:85]\n"
    "v_pk_mul_f32 v[22:23], v[22:23], v[86:87]\n"
    "ds_read_u16_d16_hi v57, v57 offset:65472\n"
    "s_add_u32 m0, s47, 4096\n"
    "v_pk_mul_f32 v[24:25], v[24:25], v[88:89]\n"
    "v_pk_mul_f32 v[26:27], v[26:27], v[90:91]\n"
    "s_waitcnt lgkmcnt(7)\n"
    "buffer_load_dwordx4 v116, s[24:27], s48 offen lds\n"
    "v_pk_mul_f32 v[28:29], v[28:29], v[92:93]\n"
    "buffer_load_dwordx4 v117, s[24:27], s48 offen offset:1024 lds\n"
    "s_add_u32 s48, s48, 64\n"
    "v_pk_fma_f32 v[80:81], v[32:33], v[32:33], s[50:51] neg_lo:[0,1,0] neg_hi:[0,1,0]\n"
    "v_pk_fma_f32 v[82:83], v[34:35], v[34:35], s[50:51] neg_lo:[0,1,0] neg_hi:[0,1,0]\n"
    "v_pk_fma_f32 v[84:85], v[36:37], v[36:37], s[50:51] neg_lo:[0,1,0] neg_hi:[0,1,0]\n"
    "ds_read_u16 v56, v56 offset:65472\n"
    "v_pk_fma_f32 v[86:87], v[38:39], v[38:39], s[50:51] neg_lo:[0,1,0] neg_hi:[0,1,0]\n"
    "v_pk_fma_f32 v[88:89], v[40:41], v[40:41], s[50:51] neg_lo:[0,1,0] neg_hi:[0,1,0]\n"
    "v_pk_fma_f32 v[90:91], v[42:43], v[42:43], s[50:51] neg_lo:[0,1,0] neg_hi:[0,1,0]\n"
    "v_pk_fma_f32 v[92:93], v[44:45], v[44:45], s[50:51] neg_lo:[0,1,0] neg_hi:[0,1,0]\n"
    "v_pk_mul_f32 v[80:81], v[80:81], v[32:33]\n"
    "v_pk_mul_f32 v[82:83], v[82:83], v[34:35]\n"
    "ds_read_u16_d16_hi v55, v55 offset:65472\n"
    "v_pk_mul_f32 v[84:85], v[84:85], v[36:37]\n"
    "v_pk_mul_f32 v[86:87], v[86:87], v[38:39]\n"
    "v_pk_mul_f32 v[88:89], v[88:89], v[40:41]\n"
    "v_pk_mul_f32 v[90:91], v[90:91], v[42:43]\n"
    "v_pk_mul_f32 v[92:93], v[92:93], v[44:45]\n"
    "v_cvt_pk_bf16_f32 v16, v16, v17\n"
    "ds_read_u16 v54, v54 offset:65472\n"
    "v_cvt_pk_bf16_f32 v17, v18, v19\n"
    "v_cvt_pk_bf16_f32 v18, v20, v21\n"
    "v_cvt_pk_bf16_f32 v19, v22, v23\n"
    "v_cvt_pk_bf16_f32 v20, v24, v25\n"
    "v_cvt_pk_bf16_f32 v21, v26, v27\n"
    "ds_read_u16_d16_hi v53, v53 offset:65472\n"
    "v_cvt_pk_bf16_f32 v22, v28, v29\n"
    "v_or_b32_e32 v23, v30, v31\n"
    "v_or_b32_e32 v108, v96, v97\n"
    "v_or_b32_e32 v109, v98, v99\n"
    "v_mfma_f32_32x32x16_bf16 a[0:15], v[16:19], a[80:83], a[0:15]\n"
    "v_exp_f32_e32 v80, v80\n"
    "ds_read_u16 v52, v52 offset:65472\n"
    "v_exp_f32_e32 v81, v81\n"
    "v_mfma_f32_32x32x16_bf16 a[0:15], v[20:23], a[84:87], a[0:15]\n"
    "v_exp_f32_e32 v82, v82\n"
    "ds_read_u16_d16_hi v51, v51 offset:65472\n"
    "v_exp_f32_e32 v83, v83\n"
    "v_exp_f32_e32 v84, v84\n"
    "v_exp_f32_e32 v85, v85\n"
    "ds_read_u16 v50, v50 offset:65472\n"
    "s_waitcnt vmcnt(4)\n"
    "ds_read_b64_tr_b16 a[72:73], v112\n"
    "v_exp_f32_e32 v86, v86\n"
    "ds_read_b64_tr_b16 a[74:75], v112 offset:512\n"
    "v_exp_f32_e32 v87, v87\n"
    "ds_read_b64_tr_b16 a[76:77], v112 offset:1024\n"
    "v_exp_f32_e64 v88, v88\n"
    "ds_read_u16_d16_hi v49, v49 offset:65472\n"
    "v_mfma_f32_16x16x16bf16_1k v[96:99], a[102:103], a[118:119], 0\n"
    "ds_read_b64_tr_b16 a[78:79], v112 offset:1536\n"
    "v_exp_f32_e32 v89, v89\n"
    "v_mfma_f32_32x32x8bf16_1k v[16:31], a[102:103], a[110:111], 0\n"
    "v_exp_f32_e32 v90, v90\n"
    "ds_read_u16 v48, v48 offset:65472\n"
    "v_exp_f32_e32 v91, v91\n"
    "v_exp_f32_e32 v92, v92\n"
    "v_exp_f32_e32 v93, v93\n"
    "s_nop 2\n"
    "v_mfma_f32_16x16x4bf16_1k v[80:95], a[122:123], a[122:123], v[80:95]\n"
    "v_lshlrev_b32_sdwa v100, 1, v100 dst_sel:DWORD dst_unused:UNUSED_PAD src0_sel:DWORD src1_sel:WORD_1\n"
    "ds_read_u16 v100, v100 offset:65472\n"
    "s_waitcnt lgkmcnt(15)\n"
    "v_or_b32_e32 v63, v62, v63\n"
    "v_lshlrev_b32_sdwa v101, 1, v101 dst_sel:DWORD dst_unused:UNUSED_PAD src0_sel:DWORD src1_sel:WORD_1\n"
    "v_or_b32_e64 v62, v60, v61\n"
    "v_lshlrev_b32_sdwa v102, 1, v102 dst_sel:DWORD dst_unused:UNUSED_PAD src0_sel:DWORD src1_sel:WORD_1\n"
    "s_mov_b32 m0, s47\n"
    "v_or_b32_e32 v61, v58, v59\n"
    "v_lshlrev_b32_sdwa v103, 1, v103 dst_sel:DWORD dst_unused:UNUSED_PAD src0_sel:DWORD src1_sel:WORD_1\n"
    "ds_read_u16_d16_hi v101, v101 offset:65472\n"
    "s_waitcnt lgkmcnt(14)\n"
    "v_or_b32_e32 v60, v56, v57\n"
    "v_lshlrev_b32_sdwa v46, 1, v46 dst_sel:DWORD dst_unused:UNUSED_PAD src0_sel:DWORD src1_sel:WORD_1\n"
    "s_waitcnt lgkmcnt(3)\n"
    "buffer_load_dwordx4 v120, s[28:31], s49 offen lds\n"
    "v_or_b32_e32 v59, v54, v55\n"
    "v_lshlrev_b32_sdwa v47, 1, v47 dst_sel:DWORD dst_unused:UNUSED_PAD src0_sel:DWORD src1_sel:WORD_1\n"
    "v_or_b32_e32 v58, v52, v53\n"
    "v_or_b32_e32 v57, v50, v51\n"
    "ds_read_u16 v102, v102 offset:65472\n"
    "buffer_load_dwordx4 v120, s[28:31], s49 offen offset:1024 lds\n"
    "s_waitcnt lgkmcnt(3)\n"
    "v_or_b32_e32 v56, v48, v49\n"
    "v_mfma_f32_32x32x16_bf16 a[16:31], v[60:63], a[84:87], a[16:31]\n"
    "s_add_u32 s49, s49, 2048\n"
    "v_lshlrev_b32_sdwa v79, 1, v79 dst_sel:DWORD dst_unused:UNUSED_PAD src0_sel:DWORD src1_sel:WORD_1\n"
    "v_lshlrev_b32_sdwa v78, 1, v78 dst_sel:DWORD dst_unused:UNUSED_PAD src0_sel:DWORD src1_sel:WORD_1\n"
    "v_lshlrev_b32_sdwa v77, 1, v77 dst_sel:DWORD dst_unused:UNUSED_PAD src0_sel:DWORD src1_sel:WORD_1\n"
    "v_mfma_f32_32x32x16_bf16 a[16:31], v[56:59], a[80:83], a[16:31]\n"
    "ds_read_u16_d16_hi v103, v103 offset:65472\n"
    "v_lshlrev_b32_sdwa v76, 1, v76 dst_sel:DWORD dst_unused:UNUSED_PAD src0_sel:DWORD src1_sel:WORD_1\n"
    "v_lshlrev_b32_sdwa v75, 1, v75 dst_sel:DWORD dst_unused:UNUSED_PAD src0_sel:DWORD src1_sel:WORD_1\n"
    "v_lshlrev_b32_sdwa v74, 1, v74 dst_sel:DWORD dst_unused:UNUSED_PAD src0_sel:DWORD src1_sel:WORD_1\n"
    "v_lshlrev_b32_sdwa v73, 1, v73 dst_sel:DWORD dst_unused:UNUSED_PAD src0_sel:DWORD src1_sel:WORD_1\n"
    "v_mfma_f32_32x32x16_bf16 v[48:63], a[72:75], a[56:59], v[16:31]\n"
    "ds_read_u16 v46, v46 offset:65472\n"
    "v_lshlrev_b32_sdwa v72, 1, v72 dst_sel:DWORD dst_unused:UNUSED_PAD src0_sel:DWORD src1_sel:WORD_1\n"
    "v_lshlrev_b32_sdwa v71, 1, v71 dst_sel:DWORD dst_unused:UNUSED_PAD src0_sel:DWORD src1_sel:WORD_1\n"
    "v_lshlrev_b32_sdwa v70, 1, v70 dst_sel:DWORD dst_unused:UNUSED_PAD src0_sel:DWORD src1_sel:WORD_1\n"
    "s_waitcnt vmcnt(4)\n"
    "ds_read_b64_tr_b16 a[80:81], v112 offset:2048\n"
    "v_lshlrev_b32_sdwa v69, 1, v69 dst_sel:DWORD dst_unused:UNUSED_PAD src0_sel:DWORD src1_sel:WORD_1\n"
    "v_mfma_f32_32x32x16_bf16 v[16:31], a[72:75], a[48:51], v[16:31]\n"
    "ds_read_b64_tr_b16 a[82:83], v112 offset:2560\n"
    "v_lshlrev_b32_sdwa v68, 1, v68 dst_sel:DWORD dst_unused:UNUSED_PAD src0_sel:DWORD src1_sel:WORD_1\n"
    "ds_read_u16_d16_hi v47, v47 offset:65472\n"
    "ds_read_b64_tr_b16 a[84:85], v112 offset:3072\n"
    "v_lshlrev_b32_sdwa v67, 1, v67 dst_sel:DWORD dst_unused:UNUSED_PAD src0_sel:DWORD src1_sel:WORD_1\n"
    "ds_read_b64_tr_b16 a[86:87], v112 offset:3584\n"
    "v_lshlrev_b32_sdwa v66, 1, v66 dst_sel:DWORD dst_unused:UNUSED_PAD src0_sel:DWORD src1_sel:WORD_1\n"
    "ds_read_b64_tr_b16 a[90:91], v113 offset:2048\n"
    "v_lshlrev_b32_sdwa v65, 1, v65 dst_sel:DWORD dst_unused:UNUSED_PAD src0_sel:DWORD src1_sel:WORD_1\n"
    "v_mfma_f32_32x32x16_bf16 v[16:31], a[76:79], a[52:55], v[16:31]\n"
    "ds_read_b64_tr_b16 a[94:95], v113 offset:2080\n"
    "v_lshlrev_b32_sdwa v64, 1, v64 dst_sel:DWORD dst_unused:UNUSED_PAD src0_sel:DWORD src1_sel:WORD_1\n"
    "ds_read_b64_tr_b16 a[126:127], v113 offset:3072\n"
    "ds_read_b64_tr_b16 v[6:7], v113 offset:3104\n"
    "v_rcp_f32_e32 v80, v80\n"
    "ds_read_u16_d16_hi v79, v79 offset:65472\n"
    "v_rcp_f32_e32 v81, v81\n"
    "v_mfma_f32_32x32x16_bf16 v[48:63], a[76:79], a[60:63], v[48:63]\n"
    "v_rcp_f32_e32 v82, v82\n"
    "ds_read_u16 v78, v78 offset:65472\n"
    "v_rcp_f32_e32 v83, v83\n"
    "v_rcp_f32_e32 v84, v84\n"
    "v_mfma_f32_16x16x32_bf16 v[96:99], a[72:75], a[64:67], v[96:99]\n"
    "ds_read_u16_d16_hi v77, v77 offset:65472\n"
    "v_rcp_f32_e64 v85, v85\n"
    "v_mfma_f32_16x16x32_bf16 v[96:99], a[76:79], a[68:71], v[96:99]\n"
    "v_rcp_f32_e32 v86, v86\n"
    "v_rcp_f32_e32 v87, v87\n"
    "ds_read_u16 v76, v76 offset:65472\n"
    "v_rcp_f32_e32 v88, v88\n"
    "v_rcp_f32_e32 v89, v89\n"
    "v_rcp_f32_e64 v90, v90\n"
    "ds_read_u16_d16_hi v75, v75 offset:65472\n"
    "v_rcp_f32_e32 v91, v91\n"
    "v_rcp_f32_e32 v92, v92\n"
    "v_rcp_f32_e32 v93, v93\n"
    "ds_read_u16 v74, v74 offset:65472\n"
    "s_cmp_lt_u32 s44, 4\n"
    "s_cbranch_scc0 Lq14%=\n"
    "s_setprio 0\n"
    "Lq14%=:\n"
    "v_pk_mul_f32 v[32:33], v[32:33], v[80:81]\n"
    "v_pk_mul_f32 v[34:35], v[34:35], v[82:83]\n"
    "v_pk_mul_f32 v[36:37], v[36:37], v[84:85]\n"
    "v_pk_mul_f32 v[38:39], v[38:39], v[86:87]\n"
    "ds_read_u16_d16_hi v73, v73 offset:65472\n"
    "s_add_u32 m0, s47, 2048\n"
    "v_pk_mul_f32 v[40:41], v[40:41], v[88:89]\n"
    "v_pk_mul_f32 v[42:43], v[42:43], v[90:91]\n"
    "s_waitcnt lgkmcnt(7)\n"
    "buffer_load_dwordx4 v116, s[24:27], s48 offen lds\n"
    "v_pk_mul_f32 v[44:45], v[44:45], v[92:93]\n"
    "buffer_load_dwordx4 v117, s[24:27], s48 offen offset:1024 lds\n"
    "s_add_u32 s48, s48, 64\n"
    "v_pk_fma_f32 v[80:81], v[16:17], v[16:17], s[50:51] neg_lo:[0,1,0] neg_hi:[0,1,0]\n"
    "v_pk_fma_f32 v[82:83], v[18:19], v[18:19], s[50:51] neg_lo:[0,1,0] neg_hi:[0,1,0]\n"
    "v_pk_fma_f32 v[84:85], v[20:21], v[20:21], s[50:51] neg_lo:[0,1,0] neg_hi:[0,1,0]\n"
    "ds_read_u16 v72, v72 offset:65472\n"
    "v_pk_fma_f32 v[86:87], v[22:23], v[22:23], s[50:51] neg_lo:[0,1,0] neg_hi:[0,1,0]\n"
    "v_pk_fma_f32 v[88:89], v[24:25], v[24:25], s[50:51] neg_lo:[0,1,0] neg_hi:[0,1,0]\n"
    "v_pk_fma_f32 v[90:91], v[26:27], v[26:27], s[50:51] neg_lo:[0,1,0] neg_hi:[0,1,0]\n"
    "v_pk_fma_f32 v[92:93], v[28:29], v[28:29], s[50:51] neg_lo:[0,1,0] neg_hi:[0,1,0]\n"
    "v_pk_mul_f32 v[80:81], v[80:81], v[16:17]\n"
    "v_pk_mul_f32 v[82:83], v[82:83], v[18:19]\n"
    "ds_read_u16_d16_hi v71, v71 offset:65472\n"
    "v_pk_mul_f32 v[84:85], v[84:85], v[20:21]\n"
    "v_pk_mul_f32 v[86:87], v[86:87], v[22:23]\n"
    "v_pk_mul_f32 v[88:89], v[88:89], v[24:25]\n"
    "v_pk_mul_f32 v[90:91], v[90:91], v[26:27]\n"
    "v_pk_mul_f32 v[92:93], v[92:93], v[28:29]\n"
    "v_cvt_pk_bf16_f32 v32, v32, v33\n"
    "ds_read_u16 v70, v70 offset:65472\n"
    "v_cvt_pk_bf16_f32 v33, v34, v35\n"
    "v_cvt_pk_bf16_f32 v34, v36, v37\n"
    "v_cvt_pk_bf16_f32 v35, v38, v39\n"
    "v_cvt_pk_bf16_f32 v36, v40, v41\n"
    "v_cvt_pk_bf16_f32 v37, v42, v43\n"
    "ds_read_u16_d16_hi v69, v69 offset:65472\n"
    "v_cvt_pk_bf16_f32 v38, v44, v45\n"
    "v_or_b32_e32 v39, v46, v47\n"
    "v_or_b32_e32 v110, v100, v101\n"
    "v_or_b32_e32 v111, v102, v103\n"
    "v_mfma_f32_32x32x16_bf16 a[0:15], v[32:35], a[80:83], a[0:15]\n"
    "v_exp_f32_e32 v80, v80\n"
    "ds_read_u16 v68, v68 offset:65472\n"
    "v_exp_f32_e32 v81, v81\n"
    "v_mfma_f32_32x32x16_bf16 a[0:15], v[36:39], a[84:87], a[0:15]\n"
    "v_exp_f32_e32 v82, v82\n"
    "ds_read_u16_d16_hi v67, v67 offset:65472\n"
    "v_exp_f32_e64 v83, v83\n"
    "v_mfma_f32_16x16x32_bf16 a[32:35], v[108:111], a[88:91], a[32:35]\n"
    "v_exp_f32_e32 v84, v84\n"
    "v_exp_f32_e32 v85, v85\n"
    "ds_read_u16 v66, v66 offset:65472\n"
    "v_mfma_f32_16x16x32_bf16 a[36:39], v[108:111], a[92:95], a[36:39]\n"
    "s_waitcnt vmcnt(4)\n"
    "ds_read_b64_tr_b16 a[72:73], v112 offset:4096\n"
    "v_exp_f32_e32 v86, v86\n"
    "v_mfma_f32_16x16x32_bf16 a[40:43], v[108:111], a[124:127], a[40:43]\n"
    "ds_read_u16_d16_hi v65, v65 offset:65472\n"
    "ds_read_b64_tr_b16 a[74:75], v112 offset:4608\n"
    "v_exp_f32_e64 v87, v87\n"
    "v_mfma_f32_16x16x32_bf16 a[44:47], v[108:111], v[4:7], a[44:47]\n"
    "ds_read_b64_tr_b16 a[76:77], v112 offset:5120\n"
    "v_exp_f32_e64 v88, v88\n"
    "v_mfma_f32_16x16x16bf16_1k v[100:103], a[102:103], a[120:121], 0\n"
    "ds_read_u16 v64, v64 offset:65472\n"
    "ds_read_b64_tr_b16 a[78:79], v112 offset:5632\n"
    "v_exp_f32_e32 v89, v89\n"
    "v_mfma_f32_32x32x8bf16_1k v[32:47], a[102:103], a[112:113], 0\n"
    "v_exp_f32_e32 v90, v90\n"
    "v_exp_f32_e32 v91, v91\n"
    "v_exp_f32_e32 v92, v92\n"
    "v_exp_f32_e32 v93, v93\n"
    "s_nop 2\n"
    "v_mfma_f32_16x16x4bf16_1k v[80:95], a[122:123], a[122:123], v[80:95]\n"
    "v_lshlrev_b32_sdwa v96, 1, v96 dst_sel:DWORD dst_unused:UNUSED_PAD src0_sel:DWORD src1_sel:WORD_1\n"
    "ds_read_u16 v96, v96 offset:65472\n"
    "s_waitcnt lgkmcnt(15)\n"
    "v_or_b32_e32 v79, v78, v79\n"
    "v_lshlrev_b32_sdwa v97, 1, v97 dst_sel:DWORD dst_unused:UNUSED_PAD src0_sel:DWORD src1_sel:WORD_1\n"
    "v_or_b32_e64 v78, v76, v77\n"
    "v_lshlrev_b32_sdwa v98, 1, v98 dst_sel:DWORD dst_unused:UNUSED_PAD src0_sel:DWORD src1_sel:WORD_1\n"
    "s_add_u32 m0, s47, 4096\n"
    "v_or_b32_e64 v77, v74, v75\n"
    "v_lshlrev_b32_sdwa v99, 1, v99 dst_sel:DWORD dst_unused:UNUSED_PAD src0_sel:DWORD src1_sel:WORD_1\n"
    "ds_read_u16_d16_hi v97, v97 offset:65472\n"
    "s_waitcnt lgkmcnt(14)\n"
    "v_or_b32_e32 v76, v72, v73\n"
    "v_lshlrev_b32_sdwa v30, 1, v30 dst_sel:DWORD dst_unused:UNUSED_PAD src0_sel:DWORD src1_sel:WORD_1\n"
    "s_waitcnt lgkmcnt(2)\n"
    "buffer_load_dwordx4 v120, s[28:31], s49 offen lds\n"
    "v_or_b32_e32 v75, v70, v71\n"
    "v_lshlrev_b32_sdwa v31, 1, v31 dst_sel:DWORD dst_unused:UNUSED_PAD src0_sel:DWORD src1_sel:WORD_1\n"
    "v_or_b32_e32 v74, v68, v69\n"
    "v_or_b32_e32 v73, v66, v67\n"
    "ds_read_u16 v98, v98 offset:65472\n"
    "buffer_load_dwordx4 v120, s[28:31], s49 offen offset:1024 lds\n"
    "v_or_b32_e32 v72, v64, v65\n"
    "v_mfma_f32_32x32x16_bf16 a[16:31], v[76:79], a[84:87], a[16:31]\n"
    "s_add_u32 s49, s49, 2048\n"
    "v_lshlrev_b32_sdwa v63, 1, v63 dst_sel:DWORD dst_unused:UNUSED_PAD src0_sel:DWORD src1_sel:WORD_1\n"
    "v_lshlrev_b32_sdwa v62, 1, v62 dst_sel:DWORD dst_unused:UNUSED_PAD src0_sel:DWORD src1_sel:WORD_1\n"
    "v_lshlrev_b32_sdwa v61, 1, v61 dst_sel:DWORD dst_unused:UNUSED_PAD src0_sel:DWORD src1_sel:WORD_1\n"
    "v_mfma_f32_32x32x16_bf16 a[16:31], v[72:75], a[80:83], a[16:31]\n"
    "ds_read_u16_d16_hi v99, v99 offset:65472\n"
    "v_lshlrev_b32_sdwa v60, 1, v60 dst_sel:DWORD dst_unused:UNUSED_PAD src0_sel:DWORD src1_sel:WORD_1\n"
    "v_lshlrev_b32_sdwa v59, 1, v59 dst_sel:DWORD dst_unused:UNUSED_PAD src0_sel:DWORD src1_sel:WORD_1\n"
    "v_lshlrev_b32_sdwa v58, 1, v58 dst_sel:DWORD dst_unused:UNUSED_PAD src0_sel:DWORD src1_sel:WORD_1\n"
    "v_lshlrev_b32_sdwa v57, 1, v57 dst_sel:DWORD dst_unused:UNUSED_PAD src0_sel:DWORD src1_sel:WORD_1\n"
    "v_mfma_f32_32x32x16_bf16 v[64:79], a[72:75], a[56:59], v[32:47]\n"
    "ds_read_u16 v30, v30 offset:65472\n"
    "v_lshlrev_b32_sdwa v56, 1, v56 dst_sel:DWORD dst_unused:UNUSED_PAD src0_sel:DWORD src1_sel:WORD_1\n"
    "v_lshlrev_b32_sdwa v55, 1, v55 dst_sel:DWORD dst_unused:UNUSED_PAD src0_sel:DWORD src1_sel:WORD_1\n"
    "v_lshlrev_b32_sdwa v54, 1, v54 dst_sel:DWORD dst_unused:UNUSED_PAD src0_sel:DWORD src1_sel:WORD_1\n"
    "s_waitcnt vmcnt(4)\n"
    "ds_read_b64_tr_b16 a[80:81], v112\n"
    "v_lshlrev_b32_sdwa v53, 1, v53 dst_sel:DWORD dst_unused:UNUSED_PAD src0_sel:DWORD src1_sel:WORD_1\n"
    "v_mfma_f32_32x32x16_bf16 v[32:47], a[72:75], a[48:51], v[32:47]\n"
    "ds_read_b64_tr_b16 a[82:83], v112 offset:512\n"
    "v_lshlrev_b32_sdwa v52, 1, v52 dst_sel:DWORD dst_unused:UNUSED_PAD src0_sel:DWORD src1_sel:WORD_1\n"
    "ds_read_u16_d16_hi v31, v31 offset:65472\n"
    "ds_read_b64_tr_b16 a[84:85], v112 offset:1024\n"
    "v_lshlrev_b32_sdwa v51, 1, v51 dst_sel:DWORD dst_unused:UNUSED_PAD src0_sel:DWORD src1_sel:WORD_1\n"
    "ds_read_b64_tr_b16 a[86:87], v112 offset:1536\n"
    "v_lshlrev_b32_sdwa v50, 1, v50 dst_sel:DWORD dst_unused:UNUSED_PAD src0_sel:DWORD src1_sel:WORD_1\n"
    "ds_read_b64_tr_b16 a[88:89], v113\n"
    "v_lshlrev_b32_sdwa v49, 1, v49 dst_sel:DWORD dst_unused:UNUSED_PAD src0_sel:DWORD src1_sel:WORD_1\n"
    "v_mfma_f32_32x32x16_bf16 v[32:47], a[76:79], a[52:55], v[32:47]\n"
    "ds_read_b64_tr_b16 a[92:93], v113 offset:32\n"
    "v_lshlrev_b32_sdwa v48, 1, v48 dst_sel:DWORD dst_unused:UNUSED_PAD src0_sel:DWORD src1_sel:WORD_1\n"
    "ds_read_b64_tr_b16 a[124:125], v113 offset:1024\n"
    "ds_read_b64_tr_b16 v[4:5], v113 offset:1056\n"
    "v_rcp_f32_e32 v80, v80\n"
    "ds_read_u16_d16_hi v63, v63 offset:65472\n"
    "v_rcp_f32_e32 v81, v81\n"
    "v_mfma_f32_32x32x16_bf16 v[64:79], a[76:79], a[60:63], v[64:79]\n"
    "v_rcp_f32_e64 v82, v82\n"
    "ds_read_u16 v62, v62 offset:65472\n"
    "v_rcp_f32_e32 v83, v83\n"
    "v_rcp_f32_e32 v84, v84\n"
    "v_mfma_f32_16x16x32_bf16 v[100:103], a[72:75], a[64:67], v[100:103]\n"
    "ds_read_u16_d16_hi v61, v61 offset:65472\n"
    "v_rcp_f32_e64 v85, v85\n"
    "v_mfma_f32_16x16x32_bf16 v[100:103], a[76:79], a[68:71], v[100:103]\n"
    "v_rcp_f32_e32 v86, v86\n"
    "v_rcp_f32_e32 v87, v87\n"
    "ds_read_u16 v60, v60 offset:65472\n"
    "v_rcp_f32_e32 v88, v88\n"
    "v_rcp_f32_e32 v89, v89\n"
    "v_rcp_f32_e64 v90, v90\n"
    "ds_read_u16_d16_hi v59, v59 offset:65472\n"
    "v_rcp_f32_e32 v91, v91\n"
    "v_rcp_f32_e32 v92, v92\n"
    "v_rcp_f32_e32 v93, v93\n"
    "ds_read_u16 v58, v58 offset:65472\n"
    "v_pk_mul_f32 v[16:17], v[16:17], v[80:81]\n"
    "v_pk_mul_f32 v[18:19], v[18:19], v[82:83]\n"
    "v_pk_mul_f32 v[20:21], v[20:21], v[84:85]\n"
    "v_pk_mul_f32 v[22:23], v[22:23], v[86:87]\n"
    "ds_read_u16_d16_hi v57, v57 offset:65472\n"
    "s_mov_b32 m0, s47\n"
    "v_pk_mul_f32 v[24:25], v[24:25], v[88:89]\n"
    "v_pk_mul_f32 v[26:27], v[26:27], v[90:91]\n"
    "s_waitcnt lgkmcnt(7)\n"
    "buffer_load_dwordx4 v116, s[24:27], s48 offen lds\n"
    "v_pk_mul_f32 v[28:29], v[28:29], v[92:93]\n"
    "buffer_load_dwordx4 v117, s[24:27], s48 offen offset:1024 lds\n"
    "s_add_u32 s48, s48, 64\n"
    "v_pk_fma_f32 v[80:81], v[32:33], v[32:33], s[50:51] neg_lo:[0,1,0] neg_hi:[0,1,0]\n"
    "v_pk_fma_f32 v[82:83], v[34:35], v[34:35], s[50:51] neg_lo:[0,1,0] neg_hi:[0,1,0]\n"
    "v_pk_fma_f32 v[84:85], v[36:37], v[36:37], s[50:51] neg_lo:[0,1,0] neg_hi:[0,1,0]\n"
    "ds_read_u16 v56, v56 offset:65472\n"
    "v_pk_fma_f32 v[86:87], v[38:39], v[38:39], s[50:51] neg_lo:[0,1,0] neg_hi:[0,1,0]\n"
    "v_pk_fma_f32 v[88:89], v[40:41], v[40:41], s[50:51] neg_lo:[0,1,0] neg_hi:[0,1,0]\n"
    "v_pk_fma_f32 v[90:91], v[42:43], v[42:43], s[50:51] neg_lo:[0,1,0] neg_hi:[0,1,0]\n"
    "v_pk_fma_f32 v[92:93], v[44:45], v[44:45], s[50:51] neg_lo:[0,1,0] neg_hi:[0,1,0]\n"
    "v_pk_mul_f32 v[80:81], v[80:81], v[32:33]\n"
    "v_pk_mul_f32 v[82:83], v[82:83], v[34:35]\n"
    "ds_read_u16_d16_hi v55, v55 offset:65472\n"
    "v_pk_mul_f32 v[84:85], v[84:85], v[36:37]\n"
    "v_pk_mul_f32 v[86:87], v[86:87], v[38:39]\n"
    "v_pk_mul_f32 v[88:89], v[88:89], v[40:41]\n"
    "v_pk_mul_f32 v[90:91], v[90:91], v[42:43]\n"
    "v_pk_mul_f32 v[92:93], v[92:93], v[44:45]\n"
    "v_cvt_pk_bf16_f32 v16, v16, v17\n"
    "ds_read_u16 v54, v54 offset:65472\n"
    "v_cvt_pk_bf16_f32 v17, v18, v19\n"
    "v_cvt_pk_bf16_f32 v18, v20, v21\n"
    "v_cvt_pk_bf16_f32 v19, v22, v23\n"
    "v_cvt_pk_bf16_f32 v20, v24, v25\n"
    "v_cvt_pk_bf16_f32 v21, v26, v27\n"
    "ds_read_u16_d16_hi v53, v53 offset:65472\n"
    "v_cvt_pk_bf16_f32 v22, v28, v29\n"
    "v_or_b32_e32 v23, v30, v31\n"
    "v_or_b32_e32 v108, v96, v97\n"
    "v_or_b32_e32 v109, v98, v99\n"
    "v_mfma_f32_32x32x16_bf16 a[0:15], v[16:19], a[80:83], a[0:15]\n"
    "v_exp_f32_e32 v80, v80\n"
    "ds_read_u16 v52, v52 offset:65472\n"
    "v_exp_f32_e32 v81, v81\n"
    "v_mfma_f32_32x32x16_bf16 a[0:15], v[20:23], a[84:87], a[0:15]\n"
    "v_exp_f32_e32 v82, v82\n"
    "ds_read_u16_d16_hi v51, v51 offset:65472\n"
    "v_exp_f32_e32 v83, v83\n"
    "v_exp_f32_e32 v84, v84\n"
    "v_exp_f32_e32 v85, v85\n"
    "ds_read_u16 v50, v50 offset:65472\n"
    "s_waitcnt vmcnt(4)\n"
    "ds_read_b64_tr_b16 a[72:73], v112 offset:2048\n"
    "v_exp_f32_e32 v86, v86\n"
    "ds_read_b64_tr_b16 a[74:75], v112 offset:2560\n"
    "v_exp_f32_e32 v87, v87\n"
    "ds_read_b64_tr_b16 a[76:77], v112 offset:3072\n"
    "v_exp_f32_e64 v88, v88\n"
    "ds_read_u16_d16_hi v49, v49 offset:65472\n"
    "v_mfma_f32_16x16x16bf16_1k v[96:99], a[104:105], a[114:115], 0\n"
    "ds_read_b64_tr_b16 a[78:79], v112 offset:3584\n"
    "v_exp_f32_e32 v89, v89\n"
    "v_mfma_f32_32x32x8bf16_1k v[16:31], a[104:105], a[106:107], 0\n"
    "v_exp_f32_e32 v90, v90\n"
    "ds_read_u16 v48, v48 offset:65472\n"
    "v_exp_f32_e32 v91, v91\n"
    "v_exp_f32_e32 v92, v92\n"
    "v_exp_f32_e32 v93, v93\n"
    "s_nop 2\n"
    "v_mfma_f32_16x16x4bf16_1k v[80:95], a[122:123], a[122:123], v[80:95]\n"
    "v_lshlrev_b32_sdwa v100, 1, v100 dst_sel:DWORD dst_unused:UNUSED_PAD src0_sel:DWORD src1_sel:WORD_1\n"
    "ds_read_u16 v100, v100 offset:65472\n"
    "s_waitcnt lgkmcnt(15)\n"
    "v_or_b32_e32 v63, v62, v63\n"
    "v_lshlrev_b32_sdwa v101, 1, v101 dst_sel:DWORD dst_unused:UNUSED_PAD src0_sel:DWORD src1_sel:WORD_1\n"
    "v_or_b32_e64 v62, v60, v61\n"
    "v_lshlrev_b32_sdwa v102, 1, v102 dst_sel:DWORD dst_unused:UNUSED_PAD src0_sel:DWORD src1_sel:WORD_1\n"
    "s_add_u32 m0, s47, 2048\n"
    "v_or_b32_e64 v61, v58, v59\n"
    "v_lshlrev_b32_sdwa v103, 1, v103 dst_sel:DWORD dst_unused:UNUSED_PAD src0_sel:DWORD src1_sel:WORD_1\n"
    "ds_read_u16_d16_hi v101, v101 offset:65472\n"
    "s_waitcnt lgkmcnt(14)\n"
    "v_or_b32_e32 v60, v56, v57\n"
    "v_lshlrev_b32_sdwa v46, 1, v46 dst_sel:DWORD dst_unused:UNUSED_PAD src0_sel:DWORD src1_sel:WORD_1\n"
    "s_waitcnt lgkmcnt(3)\n"
    "buffer_load_dwordx4 v120, s[28:31], s49 offen lds\n"
    "v_or_b32_e32 v59, v54, v55\n"
    "v_lshlrev_b32_sdwa v47, 1, v47 dst_sel:DWORD dst_unused:UNUSED_PAD src0_sel:DWORD src1_sel:WORD_1\n"
    "v_or_b32_e32 v58, v52, v53\n"
    "v_or_b32_e32 v57, v50, v51\n"
    "ds_read_u16 v102, v102 offset:65472\n"
    "buffer_load_dwordx4 v120, s[28:31], s49 offen offset:1024 lds\n"
    "s_waitcnt lgkmcnt(3)\n"
    "v_or_b32_e32 v56, v48, v49\n"
    "v_mfma_f32_32x32x16_bf16 a[16:31], v[60:63], a[84:87], a[16:31]\n"
    "s_add_u32 s49, s49, 2048\n"
    "v_lshlrev_b32_sdwa v79, 1, v79 dst_sel:DWORD dst_unused:UNUSED_PAD src0_sel:DWORD src1_sel:WORD_1\n"
    "v_lshlrev_b32_sdwa v78, 1, v78 dst_sel:DWORD dst_unused:UNUSED_PAD src0_sel:DWORD src1_sel:WORD_1\n"
    "v_lshlrev_b32_sdwa v77, 1, v77 dst_sel:DWORD dst_unused:UNUSED_PAD src0_sel:DWORD src1_sel:WORD_1\n"
    "v_mfma_f32_32x32x16_bf16 a[16:31], v[56:59], a[80:83], a[16:31]\n"
    "ds_read_u16_d16_hi v103, v103 offset:65472\n"
    "v_lshlrev_b32_sdwa v76, 1, v76 dst_sel:DWORD dst_unused:UNUSED_PAD src0_sel:DWORD src1_sel:WORD_1\n"
    "v_lshlrev_b32_sdwa v75, 1, v75 dst_sel:DWORD dst_unused:UNUSED_PAD src0_sel:DWORD src1_sel:WORD_1\n"
    "v_lshlrev_b32_sdwa v74, 1, v74 dst_sel:DWORD dst_unused:UNUSED_PAD src0_sel:DWORD src1_sel:WORD_1\n"
    "v_lshlrev_b32_sdwa v73, 1, v73 dst_sel:DWORD dst_unused:UNUSED_PAD src0_sel:DWORD src1_sel:WORD_1\n"
    "v_mfma_f32_32x32x16_bf16 v[48:63], a[72:75], a[56:59], v[16:31]\n"
    "ds_read_u16 v46, v46 offset:65472\n"
    "v_lshlrev_b32_sdwa v72, 1, v72 dst_sel:DWORD dst_unused:UNUSED_PAD src0_sel:DWORD src1_sel:WORD_1\n"
    "v_lshlrev_b32_sdwa v71, 1, v71 dst_sel:DWORD dst_unused:UNUSED_PAD src0_sel:DWORD src1_sel:WORD_1\n"
    "v_lshlrev_b32_sdwa v70, 1, v70 dst_sel:DWORD dst_unused:UNUSED_PAD src0_sel:DWORD src1_sel:WORD_1\n"
    "s_waitcnt vmcnt(4)\n"
    "ds_read_b64_tr_b16 a[80:81], v112 offset:4096\n"
    "v_lshlrev_b32_sdwa v69, 1, v69 dst_sel:DWORD dst_unused:UNUSED_PAD src0_sel:DWORD src1_sel:WORD_1\n"
    "v_mfma_f32_32x32x16_bf16 v[16:31], a[72:75], a[48:51], v[16:31]\n"
    "ds_read_b64_tr_b16 a[82:83], v112 offset:4608\n"
    "v_lshlrev_b32_sdwa v68, 1, v68 dst_sel:DWORD dst_unused:UNUSED_PAD src0_sel:DWORD src1_sel:WORD_1\n"
    "ds_read_u16_d16_hi v47, v47 offset:65472\n"
    "ds_read_b64_tr_b16 a[84:85], v112 offset:5120\n"
    "v_lshlrev_b32_sdwa v67, 1, v67 dst_sel:DWORD dst_unused:UNUSED_PAD src0_sel:DWORD src1_sel:WORD_1\n"
    "ds_read_b64_tr_b16 a[86:87], v112 offset:5632\n"
    "v_lshlrev_b32_sdwa v66, 1, v66 dst_sel:DWORD dst_unused:UNUSED_PAD src0_sel:DWORD src1_sel:WORD_1\n"
    "ds_read_b64_tr_b16 a[90:91], v113 offset:4096\n"
    "v_lshlrev_b32_sdwa v65, 1, v65 dst_sel:DWORD dst_unused:UNUSED_PAD src0_sel:DWORD src1_sel:WORD_1\n"
    "v_mfma_f32_32x32x16_bf16 v[16:31], a[76:79], a[52:55], v[16:31]\n"
    "ds_read_b64_tr_b16 a[94:95], v113 offset:4128\n"
    "v_lshlrev_b32_sdwa v64, 1, v64 dst_sel:DWORD dst_unused:UNUSED_PAD src0_sel:DWORD src1_sel:WORD_1\n"
    "ds_read_b64_tr_b16 a[126:127], v113 offset:5120\n"
    "ds_read_b64_tr_b16 v[6:7], v113 offset:5152\n"
    "v_rcp_f32_e32 v80, v80\n"
    "ds_read_u16_d16_hi v79, v79 offset:65472\n"
    "v_rcp_f32_e32 v81, v81\n"
    "v_mfma_f32_32x32x16_bf16 v[48:63], a[76:79], a[60:63], v[48:63]\n"
    "v_rcp_f32_e32 v82, v82\n"
    "ds_read_u16 v78, v78 offset:65472\n"
    "v_rcp_f32_e32 v83, v83\n"
    "v_rcp_f32_e32 v84, v84\n"
    "v_mfma_f32_16x16x32_bf16 v[96:99], a[72:75], a[64:67], v[96:99]\n"
    "ds_read_u16_d16_hi v77, v77 offset:65472\n"
    "v_rcp_f32_e64 v85, v85\n"
    "v_mfma_f32_16x16x32_bf16 v[96:99], a[76:79], a[68:71], v[96:99]\n"
    "v_rcp_f32_e32 v86, v86\n"
    "v_rcp_f32_e32 v87, v87\n"
    "ds_read_u16 v76, v76 offset:65472\n"
    "v_rcp_f32_e32 v88, v88\n"
    "v_rcp_f32_e32 v89, v89\n"
    "v_rcp_f32_e64 v90, v90\n"
    "ds_read_u16_d16_hi v75, v75 offset:65472\n"
    "v_rcp_f32_e32 v91, v91\n"
    "v_rcp_f32_e32 v92, v92\n"
    "v_rcp_f32_e32 v93, v93\n"
    "ds_read_u16 v74, v74 offset:65472\n"
    "s_cmp_lt_u32 s44, 4\n"
    "s_cbranch_scc0 Lq16%=\n"
    "s_setprio 2\n"
    "Lq16%=:\n"
    "v_pk_mul_f32 v[32:33], v[32:33], v[80:81]\n"
    "v_pk_mul_f32 v[34:35], v[34:35], v[82:83]\n"
    "v_pk_mul_f32 v[36:37], v[36:37], v[84:85]\n"
    "v_pk_mul_f32 v[38:39], v[38:39], v[86:87]\n"
    "ds_read_u16_d16_hi v73, v73 offset:65472\n"
    "v_pk_mul_f32 v[40:41], v[40:41], v[88:89]\n"
    "v_pk_mul_f32 v[42:43], v[42:43], v[90:91]\n"
    "v_pk_mul_f32 v[44:45], v[44:45], v[92:93]\n"
    "v_pk_fma_f32 v[80:81], v[16:17], v[16:17], s[50:51] neg_lo:[0,1,0] neg_hi:[0,1,0]\n"
    "v_pk_fma_f32 v[82:83], v[18:19], v[18:19], s[50:51] neg_lo:[0,1,0] neg_hi:[0,1,0]\n"
    "v_pk_fma_f32 v[84:85], v[20:21], v[20:21], s[50:51] neg_lo:[0,1,0] neg_hi:[0,1,0]\n"
    "ds_read_u16 v72, v72 offset:65472\n"
    "v_pk_fma_f32 v[86:87], v[22:23], v[22:23], s[50:51] neg_lo:[0,1,0] neg_hi:[0,1,0]\n"
    "v_pk_fma_f32 v[88:89], v[24:25], v[24:25], s[50:51] neg_lo:[0,1,0] neg_hi:[0,1,0]\n"
    "v_pk_fma_f32 v[90:91], v[26:27], v[26:27], s[50:51] neg_lo:[0,1,0] neg_hi:[0,1,0]\n"
    "v_pk_fma_f32 v[92:93], v[28:29], v[28:29], s[50:51] neg_lo:[0,1,0] neg_hi:[0,1,0]\n"
    "v_pk_mul_f32 v[80:81], v[80:81], v[16:17]\n"
    "v_pk_mul_f32 v[82:83], v[82:83], v[18:19]\n"
    "ds_read_u16_d16_hi v71, v71 offset:65472\n"
    "v_pk_mul_f32 v[84:85], v[84:85], v[20:21]\n"
    "v_pk_mul_f32 v[86:87], v[86:87], v[22:23]\n"
    "v_pk_mul_f32 v[88:89], v[88:89], v[24:25]\n"
    "v_pk_mul_f32 v[90:91], v[90:91], v[26:27]\n"
    "v_pk_mul_f32 v[92:93], v[92:93], v[28:29]\n"
    "v_cvt_pk_bf16_f32 v32, v32, v33\n"
    "ds_read_u16 v70, v70 offset:65472\n"
    "v_cvt_pk_bf16_f32 v33, v34, v35\n"
    "v_cvt_pk_bf16_f32 v34, v36, v37\n"
    "v_cvt_pk_bf16_f32 v35, v38, v39\n"
    "v_cvt_pk_bf16_f32 v36, v40, v41\n"
    "v_cvt_pk_bf16_f32 v37, v42, v43\n"
    "ds_read_u16_d16_hi v69, v69 offset:65472\n"
    "v_cvt_pk_bf16_f32 v38, v44, v45\n"
    "s_waitcnt lgkmcnt(15)\n"
    "v_or_b32_e32 v39, v46, v47\n"
    "v_or_b32_e32 v110, v100, v101\n"
    "v_or_b32_e32 v111, v102, v103\n"
    "v_mfma_f32_32x32x16_bf16 a[0:15], v[32:35], a[80:83], a[0:15]\n"
    "v_exp_f32_e32 v80, v80\n"
    "ds_read_u16 v68, v68 offset:65472\n"
    "v_exp_f32_e32 v81, v81\n"
    "v_mfma_f32_32x32x16_bf16 a[0:15], v[36:39], a[84:87], a[0:15]\n"
    "v_exp_f32_e64 v82, v82\n"
    "ds_read_u16_d16_hi v67, v67 offset:65472\n"
    "v_exp_f32_e32 v83, v83\n"
    "s_waitcnt lgkmcnt(15)\n"
    "v_mfma_f32_16x16x32_bf16 a[32:35], v[108:111], a[88:91], a[32:35]\n"
    "v_exp_f32_e64 v84, v84\n"
    "v_exp_f32_e32 v85, v85\n"
    "ds_read_u16 v66, v66 offset:65472\n"
    "v_mfma_f32_16x16x32_bf16 a[36:39], v[108:111], a[92:95], a[36:39]\n"
    "s_waitcnt vmcnt(2)\n"
    "ds_read_b64_tr_b16 a[72:73], v112\n"
    "v_exp_f32_e32 v86, v86\n"
    "s_waitcnt lgkmcnt(15)\n"
    "v_mfma_f32_16x16x32_bf16 a[40:43], v[108:111], a[124:127], a[40:43]\n"
    "ds_read_u16_d16_hi v65, v65 offset:65472\n"
    "ds_read_b64_tr_b16 a[74:75], v112 offset:512\n"
    "v_exp_f32_e64 v87, v87\n"
    "v_mfma_f32_16x16x32_bf16 a[44:47], v[108:111], v[4:7], a[44:47]\n"
    "ds_read_b64_tr_b16 a[76:77], v112 offset:1024\n"
    "v_exp_f32_e64 v88, v88\n"
    "v_mfma_f32_16x16x16bf16_1k v[100:103], a[104:105], a[116:117], 0\n"
    "ds_read_u16 v64, v64 offset:65472\n"
    "ds_read_b64_tr_b16 a[78:79], v112 offset:1536\n"
    "v_exp_f32_e32 v89, v89\n"
    "v_mfma_f32_32x32x8bf16_1k v[32:47], a[104:105], a[108:109], 0\n"
    "v_exp_f32_e32 v90, v90\n"
    "v_exp_f32_e32 v91, v91\n"
    "v_exp_f32_e32 v92, v92\n"
    "v_exp_f32_e32 v93, v93\n"
    "s_nop 2\n"
    "v_mfma_f32_16x16x4bf16_1k v[80:95], a[122:123], a[122:123], v[80:95]\n"
    "v_lshlrev_b32_sdwa v96, 1, v96 dst_sel:DWORD dst_unused:UNUSED_PAD src0_sel:DWORD src1_sel:WORD_1\n"
    "ds_read_u16 v96, v96 offset:65472\n"
    "s_waitcnt lgkmcnt(15)\n"
    "v_or_b32_e32 v79, v78, v79\n"
    "v_lshlrev_b32_sdwa v97, 1, v97 dst_sel:DWORD dst_unused:UNUSED_PAD src0_sel:DWORD src1_sel:WORD_1\n"
    "v_or_b32_e64 v78, v76, v77\n"
    "v_lshlrev_b32_sdwa v98, 1, v98 dst_sel:DWORD dst_unused:UNUSED_PAD src0_sel:DWORD src1_sel:WORD_1\n"
    "s_mov_b32 m0, s47\n"
    "v_or_b32_e32 v77, v74, v75\n"
    "v_lshlrev_b32_sdwa v99, 1, v99 dst_sel:DWORD dst_unused:UNUSED_PAD src0_sel:DWORD src1_sel:WORD_1\n"
    "ds_read_u16_d16_hi v97, v97 offset:65472\n"
    "s_waitcnt lgkmcnt(14)\n"
    "v_or_b32_e32 v76, v72, v73\n"
    "v_lshlrev_b32_sdwa v30, 1, v30 dst_sel:DWORD dst_unused:UNUSED_PAD src0_sel:DWORD src1_sel:WORD_1\n"
    "s_waitcnt lgkmcnt(2)\n"
    "buffer_load_dwordx4 v120, s[28:31], s49 offen lds\n"
    "v_or_b32_e32 v75, v70, v71\n"
    "v_lshlrev_b32_sdwa v31, 1, v31 dst_sel:DWORD dst_unused:UNUSED_PAD src0_sel:DWORD src1_sel:WORD_1\n"
    "v_or_b32_e32 v74, v68, v69\n"
    "v_or_b32_e32 v73, v66, v67\n"
    "ds_read_u16 v98, v98 offset:65472\n"
    "buffer_load_dwordx4 v120, s[28:31], s49 offen offset:1024 lds\n"
    "v_or_b32_e32 v72, v64, v65\n"
    "v_mfma_f32_32x32x16_bf16 a[16:31], v[76:79], a[84:87], a[16:31]\n"
    "s_add_u32 s49, s49, 2048\n"
    "v_lshlrev_b32_sdwa v63, 1, v63 dst_sel:DWORD dst_unused:UNUSED_PAD src0_sel:DWORD src1_sel:WORD_1\n"
    "v_lshlrev_b32_sdwa v62, 1, v62 dst_sel:DWORD dst_unused:UNUSED_PAD src0_sel:DWORD src1_sel:WORD_1\n"
    "v_lshlrev_b32_sdwa v61, 1, v61 dst_sel:DWORD dst_unused:UNUSED_PAD src0_sel:DWORD src1_sel:WORD_1\n"
    "v_mfma_f32_32x32x16_bf16 a[16:31], v[72:75], a[80:83], a[16:31]\n"
    "ds_read_u16_d16_hi v99, v99 offset:65472\n"
    "v_lshlrev_b32_sdwa v60, 1, v60 dst_sel:DWORD dst_unused:UNUSED_PAD src0_sel:DWORD src1_sel:WORD_1\n"
    "v_lshlrev_b32_sdwa v59, 1, v59 dst_sel:DWORD dst_unused:UNUSED_PAD src0_sel:DWORD src1_sel:WORD_1\n"
    "v_lshlrev_b32_sdwa v58, 1, v58 dst_sel:DWORD dst_unused:UNUSED_PAD src0_sel:DWORD src1_sel:WORD_1\n"
    "v_lshlrev_b32_sdwa v57, 1, v57 dst_sel:DWORD dst_unused:UNUSED_PAD src0_sel:DWORD src1_sel:WORD_1\n"
    "v_mfma_f32_32x32x16_bf16 v[64:79], a[72:75], a[56:59], v[32:47]\n"
    "ds_read_u16 v30, v30 offset:65472\n"
    "v_lshlrev_b32_sdwa v56, 1, v56 dst_sel:DWORD dst_unused:UNUSED_PAD src0_sel:DWORD src1_sel:WORD_1\n"
    "v_lshlrev_b32_sdwa v55, 1, v55 dst_sel:DWORD dst_unused:UNUSED_PAD src0_sel:DWORD src1_sel:WORD_1\n"
    "v_lshlrev_b32_sdwa v54, 1, v54 dst_sel:DWORD dst_unused:UNUSED_PAD src0_sel:DWORD src1_sel:WORD_1\n"
    "s_waitcnt vmcnt(2)\n"
    "ds_read_b64_tr_b16 a[80:81], v112 offset:2048\n"
    "v_lshlrev_b32_sdwa v53, 1, v53 dst_sel:DWORD dst_unused:UNUSED_PAD src0_sel:DWORD src1_sel:WORD_1\n"
    "v_mfma_f32_32x32x16_bf16 v[32:47], a[72:75], a[48:51], v[32:47]\n"
    "ds_read_b64_tr_b16 a[82:83], v112 offset:2560\n"
    "v_lshlrev_b32_sdwa v52, 1, v52 dst_sel:DWORD dst_unused:UNUSED_PAD src0_sel:DWORD src1_sel:WORD_1\n"
    "ds_read_u16_d16_hi v31, v31 offset:65472\n"
    "ds_read_b64_tr_b16 a[84:85], v112 offset:3072\n"
    "v_lshlrev_b32_sdwa v51, 1, v51 dst_sel:DWORD dst_unused:UNUSED_PAD src0_sel:DWORD src1_sel:WORD_1\n"
    "ds_read_b64_tr_b16 a[86:87], v112 offset:3584\n"
    "v_lshlrev_b32_sdwa v50, 1, v50 dst_sel:DWORD dst_unused:UNUSED_PAD src0_sel:DWORD src1_sel:WORD_1\n"
    "ds_read_b64_tr_b16 a[88:89], v113 offset:2048\n"
    "v_lshlrev_b32_sdwa v49, 1, v49 dst_sel:DWORD dst_unused:UNUSED_PAD src0_sel:DWORD src1_sel:WORD_1\n"
    "v_mfma_f32_32x32x16_bf16 v[32:47], a[76:79], a[52:55], v[32:47]\n"
    "ds_read_b64_tr_b16 a[92:93], v113 offset:2080\n"
    "v_lshlrev_b32_sdwa v48, 1, v48 dst_sel:DWORD dst_unused:UNUSED_PAD src0_sel:DWORD src1_sel:WORD_1\n"
    "ds_read_b64_tr_b16 a[124:125], v113 offset:3072\n"
    "ds_read_b64_tr_b16 v[4:5], v113 offset:3104\n"
    "v_rcp_f32_e32 v80, v80\n"
    "ds_read_u16_d16_hi v63, v63 offset:65472\n"
    "v_rcp_f32_e32 v81, v81\n"
    "v_mfma_f32_32x32x16_bf16 v[64:79], a[76:79], a[60:63], v[64:79]\n"
    "v_rcp_f32_e64 v82, v82\n"
    "ds_read_u16 v62, v62 offset:65472\n"
    "v_rcp_f32_e32 v83, v83\n"
    "v_rcp_f32_e32 v84, v84\n"
    "v_mfma_f32_16x16x32_bf16 v[100:103], a[72:75], a[64:67], v[100:103]\n"
    "ds_read_u16_d16_hi v61, v61 offset:65472\n"
    "v_rcp_f32_e64 v85, v85\n"
    "v_mfma_f32_16x16x32_bf16 v[100:103], a[76:79], a[68:71], v[100:103]\n"
    "v_rcp_f32_e32 v86, v86\n"
    "v_rcp_f32_e32 v87, v87\n"
    "ds_read_u16 v60, v60 offset:65472\n"
    "v_rcp_f32_e32 v88, v88\n"
    "v_rcp_f32_e32 v89, v89\n"
    "v_rcp_f32_e32 v90, v90\n"
    "ds_read_u16_d16_hi v59, v59 offset:65472\n"
    "v_rcp_f32_e32 v91, v91\n"
    "v_rcp_f32_e32 v92, v92\n"
    "v_rcp_f32_e32 v93, v93\n"
    "ds_read_u16 v58, v58 offset:65472\n"
    "v_pk_mul_f32 v[16:17], v[16:17], v[80:81]\n"
    "v_pk_mul_f32 v[18:19], v[18:19], v[82:83]\n"
    "v_pk_mul_f32 v[20:21], v[20:21], v[84:85]\n"
    "v_pk_mul_f32 v[22:23], v[22:23], v[86:87]\n"
    "ds_read_u16_d16_hi v57, v57 offset:65472\n"
    "v_pk_mul_f32 v[24:25], v[24:25], v[88:89]\n"
    "v_pk_mul_f32 v[26:27], v[26:27], v[90:91]\n"
    "v_pk_mul_f32 v[28:29], v[28:29], v[92:93]\n"
    "v_pk_fma_f32 v[80:81], v[32:33], v[32:33], s[50:51] neg_lo:[0,1,0] neg_hi:[0,1,0]\n"
    "v_pk_fma_f32 v[82:83], v[34:35], v[34:35], s[50:51] neg_lo:[0,1,0] neg_hi:[0,1,0]\n"
    "v_pk_fma_f32 v[84:85], v[36:37], v[36:37], s[50:51] neg_lo:[0,1,0] neg_hi:[0,1,0]\n"
    "ds_read_u16 v56, v56 offset:65472\n"
    "v_pk_fma_f32 v[86:87], v[38:39], v[38:39], s[50:51] neg_lo:[0,1,0] neg_hi:[0,1,0]\n"
    "v_pk_fma_f32 v[88:89], v[40:41], v[40:41], s[50:51] neg_lo:[0,1,0] neg_hi:[0,1,0]\n"
    "v_pk_fma_f32 v[90:91], v[42:43], v[42:43], s[50:51] neg_lo:[0,1,0] neg_hi:[0,1,0]\n"
    "v_pk_fma_f32 v[92:93], v[44:45], v[44:45], s[50:51] neg_lo:[0,1,0] neg_hi:[0,1,0]\n"
    "v_pk_mul_f32 v[80:81], v[80:81], v[32:33]\n"
    "v_pk_mul_f32 v[82:83], v[82:83], v[34:35]\n"
    "ds_read_u16_d16_hi v55, v55 offset:65472\n"
    "v_pk_mul_f32 v[84:85], v[84:85], v[36:37]\n"
    "v_pk_mul_f32 v[86:87], v[86:87], v[38:39]\n"
    "v_pk_mul_f32 v[88:89], v[88:89], v[40:41]\n"
    "v_pk_mul_f32 v[90:91], v[90:91], v[42:43]\n"
    "v_pk_mul_f32 v[92:93], v[92:93], v[44:45]\n"
    "v_cvt_pk_bf16_f32 v16, v16, v17\n"
    "ds_read_u16 v54, v54 offset:65472\n"
    "v_cvt_pk_bf16_f32 v17, v18, v19\n"
    "v_cvt_pk_bf16_f32 v18, v20, v21\n"
    "v_cvt_pk_bf16_f32 v19, v22, v23\n"
    "v_cvt_pk_bf16_f32 v20, v24, v25\n"
    "v_cvt_pk_bf16_f32 v21, v26, v27\n"
    "ds_read_u16_d16_hi v53, v53 offset:65472\n"
    "v_cvt_pk_bf16_f32 v22, v28, v29\n"
    "s_waitcnt lgkmcnt(15)\n"
    "v_or_b32_e32 v23, v30, v31\n"
    "v_or_b32_e32 v108, v96, v97\n"
    "v_or_b32_e32 v109, v98, v99\n"
    "v_mfma_f32_32x32x16_bf16 a[0:15], v[16:19], a[80:83], a[0:15]\n"
    "v_exp_f32_e32 v80, v80\n"
    "ds_read_u16 v52, v52 offset:65472\n"
    "v_exp_f32_e32 v81, v81\n"
    "v_mfma_f32_32x32x16_bf16 a[0:15], v[20:23], a[84:87], a[0:15]\n"
    "v_exp_f32_e32 v82, v82\n"
    "ds_read_u16_d16_hi v51, v51 offset:65472\n"
    "v_exp_f32_e32 v83, v83\n"
    "v_exp_f32_e32 v84, v84\n"
    "v_exp_f32_e32 v85, v85\n"
    "ds_read_u16 v50, v50 offset:65472\n"
    "v_exp_f32_e32 v86, v86\n"
    "v_exp_f32_e32 v87, v87\n"
    "v_exp_f32_e32 v88, v88\n"
    "ds_read_u16_d16_hi v49, v49 offset:65472\n"
    "v_exp_f32_e32 v89, v89\n"
    "v_exp_f32_e32 v90, v90\n"
    "v_exp_f32_e32 v91, v91\n"
    "ds_read_u16 v48, v48 offset:65472\n"
    "v_exp_f32_e64 v92, v92\n"
    "v_exp_f32_e32 v93, v93\n"
    "s_nop 2\n"
    "v_mfma_f32_16x16x4bf16_1k v[80:95], a[122:123], a[122:123], v[80:95]\n"
    "v_lshlrev_b32_sdwa v100, 1, v100 dst_sel:DWORD dst_unused:UNUSED_PAD src0_sel:DWORD src1_sel:WORD_1\n"
    "ds_read_u16 v100, v100 offset:65472\n"
    "s_waitcnt lgkmcnt(15)\n"
    "v_or_b32_e32 v63, v62, v63\n"
    "v_lshlrev_b32_sdwa v101, 1, v101 dst_sel:DWORD dst_unused:UNUSED_PAD src0_sel:DWORD src1_sel:WORD_1\n"
    "s_waitcnt lgkmcnt(13)\n"
    "v_or_b32_e32 v62, v60, v61\n"
    "v_lshlrev_b32_sdwa v102, 1, v102 dst_sel:DWORD dst_unused:UNUSED_PAD src0_sel:DWORD src1_sel:WORD_1\n"
    "s_waitcnt lgkmcnt(11)\n"
    "v_or_b32_e32 v61, v58, v59\n"
    "v_lshlrev_b32_sdwa v103, 1, v103 dst_sel:DWORD dst_unused:UNUSED_PAD src0_sel:DWORD src1_sel:WORD_1\n"
    "ds_read_u16_d16_hi v101, v101 offset:65472\n"
    "s_waitcnt lgkmcnt(10)\n"
    "v_or_b32_e32 v60, v56, v57\n"
    "v_lshlrev_b32_sdwa v46, 1, v46 dst_sel:DWORD dst_unused:UNUSED_PAD src0_sel:DWORD src1_sel:WORD_1\n"
    "s_waitcnt lgkmcnt(8)\n"
    "v_or_b32_e32 v59, v54, v55\n"
    "v_lshlrev_b32_sdwa v47, 1, v47 dst_sel:DWORD dst_unused:UNUSED_PAD src0_sel:DWORD src1_sel:WORD_1\n"
    "s_waitcnt lgkmcnt(6)\n"
    "v_or_b32_e32 v58, v52, v53\n"
    "s_waitcnt lgkmcnt(4)\n"
    "v_or_b32_e32 v57, v50, v51\n"
    "ds_read_u16 v102, v102 offset:65472\n"
    "s_waitcnt lgkmcnt(3)\n"
    "v_or_b32_e32 v56, v48, v49\n"
    "v_mfma_f32_32x32x16_bf16 a[16:31], v[60:63], a[84:87], a[16:31]\n"
    "v_lshlrev_b32_sdwa v79, 1, v79 dst_sel:DWORD dst_unused:UNUSED_PAD src0_sel:DWORD src1_sel:WORD_1\n"
    "v_lshlrev_b32_sdwa v78, 1, v78 dst_sel:DWORD dst_unused:UNUSED_PAD src0_sel:DWORD src1_sel:WORD_1\n"
    "v_lshlrev_b32_sdwa v77, 1, v77 dst_sel:DWORD dst_unused:UNUSED_PAD src0_sel:DWORD src1_sel:WORD_1\n"
    "v_mfma_f32_32x32x16_bf16 a[16:31], v[56:59], a[80:83], a[16:31]\n"
    "ds_read_u16_d16_hi v103, v103 offset:65472\n"
    "v_lshlrev_b32_sdwa v76, 1, v76 dst_sel:DWORD dst_unused:UNUSED_PAD src0_sel:DWORD src1_sel:WORD_1\n"
    "v_lshlrev_b32_sdwa v75, 1, v75 dst_sel:DWORD dst_unused:UNUSED_PAD src0_sel:DWORD src1_sel:WORD_1\n"
    "v_lshlrev_b32_sdwa v74, 1, v74 dst_sel:DWORD dst_unused:UNUSED_PAD src0_sel:DWORD src1_sel:WORD_1\n"
    "v_lshlrev_b32_sdwa v73, 1, v73 dst_sel:DWORD dst_unused:UNUSED_PAD src0_sel:DWORD src1_sel:WORD_1\n"
    "v_lshlrev_b32_sdwa v72, 1, v72 dst_sel:DWORD dst_unused:UNUSED_PAD src0_sel:DWORD src1_sel:WORD_1\n"
    "ds_read_u16 v46, v46 offset:65472\n"
    "v_lshlrev_b32_sdwa v71, 1, v71 dst_sel:DWORD dst_unused:UNUSED_PAD src0_sel:DWORD src1_sel:WORD_1\n"
    "v_lshlrev_b32_sdwa v70, 1, v70 dst_sel:DWORD dst_unused:UNUSED_PAD src0_sel:DWORD src1_sel:WORD_1\n"
    "s_waitcnt vmcnt(0)\n"
    "ds_read_b64_tr_b16 a[80:81], v112\n"
    "v_lshlrev_b32_sdwa v69, 1, v69 dst_sel:DWORD dst_unused:UNUSED_PAD src0_sel:DWORD src1_sel:WORD_1\n"
    "ds_read_b64_tr_b16 a[82:83], v112 offset:512\n"
    "v_lshlrev_b32_sdwa v68, 1, v68 dst_sel:DWORD dst_unused:UNUSED_PAD src0_sel:DWORD src1_sel:WORD_1\n"
    "ds_read_b64_tr_b16 a[84:85], v112 offset:1024\n"
    "v_lshlrev_b32_sdwa v67, 1, v67 dst_sel:DWORD dst_unused:UNUSED_PAD src0_sel:DWORD src1_sel:WORD_1\n"
    "ds_read_b64_tr_b16 a[86:87], v112 offset:1536\n"
    "v_lshlrev_b32_sdwa v66, 1, v66 dst_sel:DWORD dst_unused:UNUSED_PAD src0_sel:DWORD src1_sel:WORD_1\n"
    "ds_read_u16_d16_hi v47, v47 offset:65472\n"
    "ds_read_b64_tr_b16 a[90:91], v113\n"
    "v_lshlrev_b32_sdwa v65, 1, v65 dst_sel:DWORD dst_unused:UNUSED_PAD src0_sel:DWORD src1_sel:WORD_1\n"
    "ds_read_b64_tr_b16 a[94:95], v113 offset:32\n"
    "v_lshlrev_b32_sdwa v64, 1, v64 dst_sel:DWORD dst_unused:UNUSED_PAD src0_sel:DWORD src1_sel:WORD_1\n"
    "ds_read_b64_tr_b16 a[126:127], v113 offset:1024\n"
    "ds_read_b64_tr_b16 v[6:7], v113 offset:1056\n"
    "v_rcp_f32_e32 v80, v80\n"
    "v_rcp_f32_e32 v81, v81\n"
    "ds_read_u16_d16_hi v79, v79 offset:65472\n"
    "v_rcp_f32_e32 v82, v82\n"
    "v_rcp_f32_e32 v83, v83\n"
    "v_rcp_f32_e32 v84, v84\n"
    "ds_read_u16 v78, v78 offset:65472\n"
    "v_rcp_f32_e32 v85, v85\n"
    "v_rcp_f32_e32 v86, v86\n"
    "v_rcp_f32_e32 v87, v87\n"
    "ds_read_u16_d16_hi v77, v77 offset:65472\n"
    "v_rcp_f32_e32 v88, v88\n"
    "v_rcp_f32_e32 v89, v89\n"
    "v_rcp_f32_e32 v90, v90\n"
    "ds_read_u16 v76, v76 offset:65472\n"
    "v_rcp_f32_e32 v91, v91\n"
    "v_rcp_f32_e32 v92, v92\n"
    "v_rcp_f32_e32 v93, v93\n"
    "ds_read_u16_d16_hi v75, v75 offset:65472\n"
    "s_cmp_lt_u32 s44, 4\n"
    "s_cbranch_scc0 Lq18%=\n"
    "s_setprio 0\n"
    "Lq18%=:\n"
    "v_pk_mul_f32 v[32:33], v[32:33], v[80:81]\n"
    "v_pk_mul_f32 v[34:35], v[34:35], v[82:83]\n"
    "v_pk_mul_f32 v[36:37], v[36:37], v[84:85]\n"
    "v_pk_mul_f32 v[38:39], v[38:39], v[86:87]\n"
    "v_pk_mul_f32 v[40:41], v[40:41], v[88:89]\n"
    "ds_read_u16 v74, v74 offset:65472\n"
    "v_pk_mul_f32 v[42:43], v[42:43], v[90:91]\n"
    "v_pk_mul_f32 v[44:45], v[44:45], v[92:93]\n"
    "v_cvt_pk_bf16_f32 v32, v32, v33\n"
    "v_cvt_pk_bf16_f32 v33, v34, v35\n"
    "ds_read_u16_d16_hi v73, v73 offset:65472\n"
    "v_cvt_pk_bf16_f32 v34, v36, v37\n"
    "v_cvt_pk_bf16_f32 v35, v38, v39\n"
    "v_cvt_pk_bf16_f32 v36, v40, v41\n"
    "v_cvt_pk_bf16_f32 v37, v42, v43\n"
    "ds_read_u16 v72, v72 offset:65472\n"
    "v_cvt_pk_bf16_f32 v38, v44, v45\n"
    "s_waitcnt lgkmcnt(12)\n"
    "v_or_b32_e32 v39, v46, v47\n"
    "v_or_b32_e32 v110, v100, v101\n"
    "v_or_b32_e32 v111, v102, v103\n"
    "ds_read_u16_d16_hi v71, v71 offset:65472\n"
    "v_mfma_f32_32x32x16_bf16 a[0:15], v[32:35], a[80:83], a[0:15]\n"
    "v_mfma_f32_32x32x16_bf16 a[0:15], v[36:39], a[84:87], a[0:15]\n"
    "s_waitcnt lgkmcnt(12)\n"
    "v_mfma_f32_16x16x32_bf16 a[32:35], v[108:111], a[88:91], a[32:35]\n"
    "s_waitcnt lgkmcnt(11)\n"
    "v_mfma_f32_16x16x32_bf16 a[36:39], v[108:111], a[92:95], a[36:39]\n"
    "ds_read_u16 v70, v70 offset:65472\n"
    "s_waitcnt lgkmcnt(11)\n"
    "v_mfma_f32_16x16x32_bf16 a[40:43], v[108:111], a[124:127], a[40:43]\n"
    "s_waitcnt lgkmcnt(10)\n"
    "v_mfma_f32_16x16x32_bf16 a[44:47], v[108:111], v[4:7], a[44:47]\n"
    "v_and_b32_e32 v48, 63, %2\n"
    "v_lshrrev_b32_e32 v50, 4, %2\n"
    "ds_read_u16_d16_hi v69, v69 offset:65472\n"
    "v_and_b32_e32 v49, 15, %2\n"
    "v_bfe_u32 v51, v48, 2, 2\n"
    "v_lshlrev_b32_e32 v54, 1, v50\n"
    "buffer_load_ushort v121, v54, s[36:39], 0 offen\n"
    "v_lshlrev_b32_e32 v55, 1, v51\n"
    "ds_read_u16 v68, v68 offset:65472\n"
    "s_lshl_b32 s55, s44, 3\n"
    "buffer_load_ushort v122, v55, s[36:39], s55 offen\n"
    "v_lshrrev_b32_e32 v52, 5, v48\n"
    "v_and_b32_e32 v53, 3, v48\n"
    "v_and_b32_e32 v56, 31, v48\n"
    "v_and_b32_e32 v57, 7, v48\n"
    "ds_read_u16_d16_hi v67, v67 offset:65472\n"
    "v_xor_b32_e32 v57, v52, v57\n"
    "v_lshlrev_b32_e32 v56, 7, v56\n"
    "v_lshl_add_u32 v56, v57, 4, v56\n"
    "s_lshl_b32 s52, s44, 12\n"
    "s_add_u32 s52, s52, 49152\n"
    "v_add_u32_e32 v114, s47, v56\n"
    "ds_read_u16 v66, v66 offset:65472\n"
    "v_add_u32_e64 v118, s52, v56\n"
    "v_bfe_u32 v57, v48, 1, 2\n"
    "v_lshrrev_b32_e32 v58, 4, v48\n"
    "v_xor_b32_e32 v57, v57, v58\n"
    "ds_read_u16_d16_hi v65, v65 offset:65472\n"
    "s_add_u32 s52, s47, 4096\n"
    "v_lshlrev_b32_e64 v56, 6, v49\n"
    "v_lshl_add_u32 v56, v57, 4, v56\n"
    "v_add_u32_e32 v115, s52, v56\n"
    "v_xor_b32_e32 v56, v49, v50\n"
    "ds_read_u16 v64, v64 offset:65472\n"
    "v_cmp_gt_u32_e32 vcc, 8, v49\n"
    "v_and_b32_e32 v56, 7, v56\n"
    "v_mov_b32_e32 v57, 49152\n"
    "v_mov_b32_e32 v58, 4096\n"
    "v_mov_b32_e32 v59, 6144\n"
    "v_lshlrev_b32_e64 v56, 4, v56\n"
    "v_lshl_add_u32 v56, v50, 7, v56\n"
    "v_cndmask_b32_e64 v57, v57, 0, vcc\n"
    "v_cndmask_b32_e32 v123, v58, v59, vcc\n"
    "v_add_u32_e32 v119, v57, v56\n"
    "s_lshl_b32 s52, s44, 2\n"
    "v_add_u32_e32 v56, s52, v51\n"
    "v_bfe_u32 v57, v56, 1, 2\n"
    "v_xor_b32_e32 v57, v57, v53\n"
    "v_lshlrev_b32_e32 v57, 4, v57\n"
    "v_lshl_add_u32 v57, v56, 6, v57\n"
    "v_add_u32_e32 v125, 4096, v57\n"
    "v_mul_u32_u24_e32 v126, 1152, v50\n"
    "v_mul_u32_u24_e32 v56, 1152, v56\n"
    "v_and_b32_e64 v57, 1, v48\n"
    "v_lshl_add_u32 v126, v49, 3, v126\n"
    "v_lshl_add_u32 v127, v57, 3, v56\n"
    "s_waitcnt lgkmcnt(14)\n"
    "v_or_b32_e32 v79, v78, v79\n"
    "s_waitcnt lgkmcnt(12)\n"
    "v_or_b32_e32 v78, v76, v77\n"
    "s_waitcnt lgkmcnt(10)\n"
    "v_or_b32_e32 v77, v74, v75\n"
    "s_waitcnt lgkmcnt(8)\n"
    "v_or_b32_e32 v76, v72, v73\n"
    "s_waitcnt lgkmcnt(6)\n"
    "v_or_b32_e32 v75, v70, v71\n"
    "s_waitcnt lgkmcnt(4)\n"
    "v_or_b32_e32 v74, v68, v69\n"
    "s_waitcnt lgkmcnt(2)\n"
    "v_or_b32_e32 v73, v66, v67\n"
    "s_waitcnt lgkmcnt(0)\n"
    "v_or_b32_e32 v72, v64, v65\n"
    "v_mfma_f32_32x32x16_bf16 a[16:31], v[76:79], a[84:87], a[16:31]\n"
    "s_nop 0\n"
    "v_mfma_f32_32x32x16_bf16 a[16:31], v[72:75], a[80:83], a[16:31]\n"
    "s_setprio 0\n"
    "ds_write_b128 v114, a[0:3]\n"
    "v_xor_b32_e32 v17, 32, v114\n"
    "ds_write_b128 v17, a[4:7]\n"
    "v_xor_b32_e32 v18, 64, v114\n"
    "ds_write_b128 v18, a[8:11]\n"
    "v_xor_b32_e32 v19, 96, v114\n"
    "ds_write_b128 v19, a[12:15]\n"
    "s_mov_b32 exec_hi, 0\n"
    "ds_write_b128 v115, a[32:35]\n"
    "ds_write_b128 v115, a[36:39] offset:1024\n"
    "s_not_b64 exec, exec\n"
    "ds_write_b128 v115, a[40:43]\n"
    "ds_write_b128 v115, a[44:47] offset:1024\n"
    "s_mov_b64 exec, -1\n"
    "v_mov_b32_e32 v8, v119\n"
    "v_add_u32_e32 v9, v8, v123\n"
    "v_add_u32_e32 v10, v9, v123\n"
    "v_add_u32_e32 v11, v10, v123\n"
    "v_add_u32_e32 v12, v11, v123\n"
    "v_add_u32_e32 v13, v12, v123\n"
    "v_add_u32_e32 v14, v13, v123\n"
    "v_add_u32_e32 v15, v14, v123\n"
    "s_waitcnt vmcnt(1)\n"
    "v_lshlrev_b32_e32 v121, 16, v121\n"
    "s_waitcnt vmcnt(0)\n"
    "v_lshlrev_b32_e32 v122, 16, v122\n"
    "s_mov_b32 s59, 0x40088e99\n"
    "v_xor_b32_e32 v21, 32, v118\n"
    "v_xor_b32_e32 v22, 64, v118\n"
    "v_xor_b32_e32 v23, 96, v118\n"
    "s_barrier\n"
    "ds_write_b128 v118, a[16:19]\n"
    "ds_write_b128 v21, a[20:23]\n"
    "ds_write_b128 v22, a[24:27]\n"
    "ds_write_b128 v23, a[28:31]\n"
    "s_waitcnt lgkmcnt(0)\n"
    "s_barrier\n"
    "ds_read_b128 v[16:19], v8\n"
    "ds_read_b128 v[20:23], v9\n"
    "ds_read_b128 v[24:27], v10\n"
    "ds_read_b128 v[28:31], v11\n"
    "ds_read_b128 v[32:35], v12\n"
    "ds_read_b128 v[36:39], v13\n"
    "ds_read_b128 v[40:43], v14\n"
    "ds_read_b128 v[44:47], v15\n"
    "ds_read_b128 v[48:51], v125\n"
    "ds_read_b128 v[52:55], v125 offset:6144\n"
    "ds_read_b128 v[56:59], v125 offset:12288\n"
    "ds_read_b128 v[60:63], v125 offset:18432\n"
    "ds_read_b128 v[64:67], v125 offset:24576\n"
    "ds_read_b128 v[68:71], v125 offset:30720\n"
    "ds_read_b128 v[72:75], v125 offset:36864\n"
    "ds_read_b128 v[76:79], v125 offset:43008\n"
    "s_waitcnt lgkmcnt(14)\n"
    "v_pk_add_f32 v[16:17], v[16:17], v[20:21]\n"
    "v_pk_add_f32 v[18:19], v[18:19], v[22:23]\n"
    "s_waitcnt lgkmcnt(12)\n"
    "v_pk_add_f32 v[24:25], v[24:25], v[28:29]\n"
    "v_pk_add_f32 v[26:27], v[26:27], v[30:31]\n"
    "s_waitcnt lgkmcnt(10)\n"
    "v_pk_add_f32 v[32:33], v[32:33], v[36:37]\n"
    "v_pk_add_f32 v[34:35], v[34:35], v[38:39]\n"
    "s_waitcnt lgkmcnt(8)\n"
    "v_pk_add_f32 v[40:41], v[40:41], v[44:45]\n"
    "v_pk_add_f32 v[42:43], v[42:43], v[46:47]\n"
    "v_pk_add_f32 v[16:17], v[16:17], v[24:25]\n"
    "v_pk_add_f32 v[18:19], v[18:19], v[26:27]\n"
    "v_pk_add_f32 v[32:33], v[32:33], v[40:41]\n"
    "v_pk_add_f32 v[34:35], v[34:35], v[42:43]\n"
    "v_pk_add_f32 v[16:17], v[16:17], v[32:33]\n"
    "v_pk_add_f32 v[18:19], v[18:19], v[34:35]\n"
    "v_fma_f32 v16, v16, s59, v121\n"
    "v_fma_f32 v17, v17, s59, v121\n"
    "v_fma_f32 v18, v18, s59, v121\n"
    "v_fma_f32 v19, v19, s59, v121\n"
    "v_cvt_pk_bf16_f32 v20, v16, v17\n"
    "v_cvt_pk_bf16_f32 v21, v18, v19\n"
    "buffer_store_dwordx2 v[20:21], v126, s[40:43], s53 offen\n"
    "s_waitcnt lgkmcnt(6)\n"
    "v_pk_add_f32 v[48:49], v[48:49], v[52:53]\n"
    "v_pk_add_f32 v[50:51], v[50:51], v[54:55]\n"
    "s_waitcnt lgkmcnt(4)\n"
    "v_pk_add_f32 v[56:57], v[56:57], v[60:61]\n"
    "v_pk_add_f32 v[58:59], v[58:59], v[62:63]\n"
    "s_waitcnt lgkmcnt(2)\n"
    "v_pk_add_f32 v[64:65], v[64:65], v[68:69]\n"
    "v_pk_add_f32 v[66:67], v[66:67], v[70:71]\n"
    "s_waitcnt lgkmcnt(0)\n"
    "v_pk_add_f32 v[72:73], v[72:73], v[76:77]\n"
    "v_pk_add_f32 v[74:75], v[74:75], v[78:79]\n"
    "v_pk_add_f32 v[48:49], v[48:49], v[56:57]\n"
    "v_pk_add_f32 v[50:51], v[50:51], v[58:59]\n"
    "v_pk_add_f32 v[64:65], v[64:65], v[72:73]\n"
    "v_pk_add_f32 v[66:67], v[66:67], v[74:75]\n"
    "v_pk_add_f32 v[48:49], v[48:49], v[64:65]\n"
    "v_pk_add_f32 v[50:51], v[50:51], v[66:67]\n"
    "s_nop 0\n"
    "v_add_f32_dpp v48, v48, v48 quad_perm:[2,3,0,1] row_mask:0xf bank_mask:0xf\n"
    "v_add_f32_dpp v49, v49, v49 quad_perm:[2,3,0,1] row_mask:0xf bank_mask:0xf\n"
    "v_add_f32_dpp v50, v50, v50 quad_perm:[2,3,0,1] row_mask:0xf bank_mask:0xf\n"
    "v_add_f32_dpp v51, v51, v51 quad_perm:[2,3,0,1] row_mask:0xf bank_mask:0xf\n"
    "v_fma_f32 v48, v48, s59, v122\n"
    "v_fma_f32 v49, v49, s59, v122\n"
    "v_fma_f32 v50, v50, s59, v122\n"
    "v_fma_f32 v51, v51, s59, v122\n"
    "v_cvt_pk_bf16_f32 v52, v48, v49\n"
    "v_cvt_pk_bf16_f32 v53, v50, v51\n"
    "s_mov_b64 exec, 0x3333\n"
    "buffer_store_dwordx2 v[52:53], v127, s[40:43], s53 offen offset:128\n"
    "s_mov_b64 exec, -1\n"
    "s_branch Lfin%=\n"
    "Lfb%=:\n"
    "s_nop 7\n"
    "v_lshlrev_b32_e32 v40, 16, v16\n"
    "v_and_b32_e32 v41, s56, v16\n"
    "v_lshlrev_b32_e32 v42, 16, v17\n"
    "v_and_b32_e32 v43, s56, v17\n"
    "v_lshlrev_b32_e32 v44, 16, v18\n"
    "v_and_b32_e32 v45, s56, v18\n"
    "v_lshlrev_b32_e32 v46, 16, v19\n"
    "v_and_b32_e32 v47, s56, v19\n"
    "v_lshlrev_b32_e32 v48, 16, v20\n"
    "v_and_b32_e32 v49, s56, v20\n"
    "v_lshlrev_b32_e32 v50, 16, v21\n"
    "v_and_b32_e32 v51, s56, v21\n"
    "v_lshlrev_b32_e32 v52, 16, v22\n"
    "v_and_b32_e32 v53, s56, v22\n"
    "v_lshlrev_b32_e32 v54, 16, v23\n"
    "v_and_b32_e32 v55, s56, v23\n"
    "v_lshlrev_b32_e32 v56, 16, v24\n"
    "v_and_b32_e32 v57, s56, v24\n"
    "v_lshlrev_b32_e32 v58, 16, v25\n"
    "v_and_b32_e32 v59, s56, v25\n"
    "v_lshlrev_b32_e32 v60, 16, v26\n"
    "v_and_b32_e32 v61, s56, v26\n"
    "v_lshlrev_b32_e32 v62, 16, v27\n"
    "v_and_b32_e32 v63, s56, v27\n"
    "v_lshlrev_b32_e32 v64, 16, v28\n"
    "v_and_b32_e32 v65, s56, v28\n"
    "v_lshlrev_b32_e32 v66, 16, v29\n"
    "v_and_b32_e32 v67, s56, v29\n"
    "v_lshlrev_b32_e32 v68, 16, v30\n"
    "v_and_b32_e32 v69, s56, v30\n"
    "v_lshlrev_b32_e32 v70, 16, v31\n"
    "v_and_b32_e32 v71, s56, v31\n"
    "v_lshlrev_b32_e32 v72, 16, v32\n"
    "v_and_b32_e32 v73, s56, v32\n"
    "v_lshlrev_b32_e32 v74, 16, v33\n"
    "v_and_b32_e32 v75, s56, v33\n"
    "v_pk_add_f32 v[76:77], v[40:41], v[42:43]\n"
    "v_pk_add_f32 v[78:79], v[44:45], v[46:47]\n"
    "v_pk_add_f32 v[76:77], v[76:77], v[48:49]\n"
    "v_pk_add_f32 v[78:79], v[78:79], v[50:51]\n"
    "v_pk_add_f32 v[76:77], v[76:77], v[52:53]\n"
    "v_pk_add_f32 v[78:79], v[78:79], v[54:55]\n"
    "v_pk_add_f32 v[76:77], v[76:77], v[56:57]\n"
    "v_pk_add_f32 v[78:79], v[78:79], v[58:59]\n"
    "v_pk_add_f32 v[76:77], v[76:77], v[60:61]\n"
    "v_pk_add_f32 v[78:79], v[78:79], v[62:63]\n"
    "v_pk_add_f32 v[76:77], v[76:77], v[64:65]\n"
    "v_pk_add_f32 v[78:79], v[78:79], v[66:67]\n"
    "v_pk_add_f32 v[76:77], v[76:77], v[68:69]\n"
    "v_pk_add_f32 v[78:79], v[78:79], v[70:71]\n"
    "v_pk_add_f32 v[76:77], v[76:77], v[72:73]\n"
    "v_pk_add_f32 v[78:79], v[78:79], v[74:75]\n"
    "v_pk_add_f32 v[76:77], v[76:77], v[78:79]\n"
    "v_add_f32_e32 v80, v76, v77\n"
    "s_nop 1\n"
    "v_add_f32_dpp v80, v80, v80 row_ror:8 row_mask:0xf bank_mask:0xf\n"
    "s_nop 1\n"
    "v_add_f32_dpp v80, v80, v80 row_ror:4 row_mask:0xf bank_mask:0xf\n"
    "s_nop 1\n"
    "v_add_f32_dpp v80, v80, v80 row_ror:2 row_mask:0xf bank_mask:0xf\n"
    "s_nop 1\n"
    "v_add_f32_dpp v80, v80, v80 row_ror:1 row_mask:0xf bank_mask:0xf\n"
    "v_mul_f32_e32 v80, 0xbae38e39, v80\n"
    "v_mov_b32_e32 v81, v80\n"
    "v_pk_add_f32 v[40:41], v[40:41], v[80:81]\n"
    "v_pk_add_f32 v[42:43], v[42:43], v[80:81]\n"
    "v_pk_add_f32 v[44:45], v[44:45], v[80:81]\n"
    "v_pk_add_f32 v[46:47], v[46:47], v[80:81]\n"
    "v_pk_add_f32 v[48:49], v[48:49], v[80:81]\n"
    "v_pk_add_f32 v[50:51], v[50:51], v[80:81]\n"
    "v_pk_add_f32 v[52:53], v[52:53], v[80:81]\n"
    "v_pk_add_f32 v[54:55], v[54:55], v[80:81]\n"
    "v_pk_add_f32 v[56:57], v[56:57], v[80:81]\n"
    "v_pk_add_f32 v[58:59], v[58:59], v[80:81]\n"
    "v_pk_add_f32 v[60:61], v[60:61], v[80:81]\n"
    "v_pk_add_f32 v[62:63], v[62:63], v[80:81]\n"
    "v_pk_add_f32 v[64:65], v[64:65], v[80:81]\n"
    "v_pk_add_f32 v[66:67], v[66:67], v[80:81]\n"
    "v_pk_add_f32 v[68:69], v[68:69], v[80:81]\n"
    "v_pk_add_f32 v[70:71], v[70:71], v[80:81]\n"
    "v_pk_add_f32 v[72:73], v[72:73], v[80:81]\n"
    "v_pk_add_f32 v[74:75], v[74:75], v[80:81]\n"
    "v_pk_mul_f32 v[76:77], v[40:41], v[40:41]\n"
    "v_pk_mul_f32 v[78:79], v[42:43], v[42:43]\n"
    "v_pk_fma_f32 v[76:77], v[44:45], v[44:45], v[76:77]\n"
    "v_pk_fma_f32 v[78:79], v[46:47], v[46:47], v[78:79]\n"
    "v_pk_fma_f32 v[76:77], v[48:49], v[48:49], v[76:77]\n"
    "v_pk_fma_f32 v[78:79], v[50:51], v[50:51], v[78:79]\n"
    "v_pk_fma_f32 v[76:77], v[52:53], v[52:53], v[76:77]\n"
    "v_pk_fma_f32 v[78:79], v[54:55], v[54:55], v[78:79]\n"
    "v_pk_fma_f32 v[76:77], v[56:57], v[56:57], v[76:77]\n"
    "v_pk_fma_f32 v[78:79], v[58:59], v[58:59], v[78:79]\n"
    "v_pk_fma_f32 v[76:77], v[60:61], v[60:61], v[76:77]\n"
    "v_pk_fma_f32 v[78:79], v[62:63], v[62:63], v[78:79]\n"
    "v_pk_fma_f32 v[76:77], v[64:65], v[64:65], v[76:77]\n"
    "v_pk_fma_f32 v[78:79], v[66:67], v[66:67], v[78:79]\n"
    "v_pk_fma_f32 v[76:77], v[68:69], v[68:69], v[76:77]\n"
    "v_pk_fma_f32 v[78:79], v[70:71], v[70:71], v[78:79]\n"
    "v_pk_fma_f32 v[76:77], v[72:73], v[72:73], v[76:77]\n"
    "v_pk_fma_f32 v[78:79], v[74:75], v[74:75], v[78:79]\n"
    "v_pk_add_f32 v[76:77], v[76:77], v[78:79]\n"
    "v_add_f32_e32 v82, v76, v77\n"
    "s_nop 1\n"
    "v_add_f32_dpp v82, v82, v82 row_ror:8 row_mask:0xf bank_mask:0xf\n"
    "s_nop 1\n"
    "v_add_f32_dpp v82, v82, v82 row_ror:4 row_mask:0xf bank_mask:0xf\n"
    "s_nop 1\n"
    "v_add_f32_dpp v82, v82, v82 row_ror:2 row_mask:0xf bank_mask:0xf\n"
    "s_nop 1\n"
    "v_add_f32_dpp v82, v82, v82 row_ror:1 row_mask:0xf bank_mask:0xf\n"
    "v_mul_f32_e32 v82, s57, v82\n"
    "v_add_f32_e32 v82, s58, v82\n"
    "v_rsq_f32_e32 v82, v82\n"
    "s_nop 0\n"
    "v_mul_f32_e32 v82, s62, v82\n"
    "v_mul_f32_e32 v80, v80, v82\n"
    "s_nop 1\n"
    "s_branch Lret%=\n"
    "Lfin%=:\n"
    :
    : "s"(ka), "s"(bid), "v"(tid), "s"(lds), "s"(cells)
    : "v4","v5","v6","v7","v8","v9","v10","v11","v12","v13","v14","v15","v16","v17","v18","v19","v20","v21","v22","v23","v24","v25","v26","v27","v28","v29","v30","v31","v32","v33","v34","v35","v36","v37","v38","v39","v40","v41","v42","v43","v44","v45","v46","v47","v48","v49","v50","v51","v52","v53","v54","v55","v56","v57","v58","v59","v60","v61","v62","v63","v64","v65","v66","v67","v68","v69","v70","v71","v72","v73","v74","v75","v76","v77","v78","v79","v80","v81","v82","v83","v84","v85","v86","v87","v88","v89","v90","v91","v92","v93","v94","v95","v96","v97","v98","v99","v100","v101","v102","v103","v104","v105","v106","v107","v108","v109","v110","v111","v112","v113","v114","v115","v116","v117","v118","v119","v120","v121","v122","v123","v124","v125","v126","v127","a0","a1","a2","a3","a4","a5","a6","a7","a8","a9","a10","a11","a12","a13","a14","a15","a16","a17","a18","a19","a20","a21","a22","a23","a24","a25","a26","a27","a28","a29","a30","a31","a32","a33","a34","a35","a36","a37","a38","a39","a40","a41","a42","a43","a44","a45","a46","a47","a48","a49","a50","a51","a52","a53","a54","a55","a56","a57","a58","a59","a60","a61","a62","a63","a64","a65","a66","a67","a68","a69","a70","a71","a72","a73","a74","a75","a76","a77","a78","a79","a80","a81","a82","a83","a84","a85","a86","a87","a88","a89","a90","a91","a92","a93","a94","a95","a96","a97","a98","a99","a100","a101","a102","a103","a104","a105","a106","a107","a108","a109","a110","a111","a112","a113","a114","a115","a116","a117","a118","a119","a120","a121","a122","a123","a124","a125","a126","a127","s20","s21","s22","s23","s24","s25","s26","s27","s28","s29","s30","s31","s32","s33","s34","s35","s36","s37","s38","s39","s40","s41","s42","s43","s44","s45","s46","s47","s48","s49","s50","s51","s52","s53","s54","s55","s56","s57","s58","s59","s60","s61","s62","s63","s64","s65","s66","s67","s68","s69","s70","s71","s72","s73","s74","s75","s76","s77","s78","s79","s80","s81","s82","s83","s84","s85","s86","s87","s88","s89","s90","s91","s92","s93","s94","s95","s96","s97","s98","s99","s100","s101","vcc","scc","memory");
}

extern "C" void kernel_launch(void* const* d_in, const int* in_sizes, int n_in,
                              void* d_out, int out_size, void* d_ws, size_t ws_size,
                              hipStream_t stream) {
  mixer_token_mlp_kernel<<<256, 512, 0, stream>>>(d_in[0], d_in[1], d_in[2], d_in[3], d_in[4], d_out);
}
```
